```python
import torch

def _softmax(x, axis=-1):
    return torch.softmax(x, dim=axis)
def _gelu(x):
    return torch.nn.functional.gelu(x, approximate="tanh")
def _silu(x):
    return torch.nn.functional.silu(x)
def _sigmoid(x):
    return torch.sigmoid(x)
def _layernorm(x, axis=-1, eps=1e-5):
    m = x.mean(dim=axis, keepdim=True)
    v = x.var(dim=axis, keepdim=True, unbiased=False)
    return (x - m) / torch.sqrt(v + eps)
def _scatter_add(tgt, idx, src, axis=0):
    return tgt.clone().index_add_(axis, idx, src)
def _segment_sum(src, seg, n):
    out = torch.zeros((n,) + src.shape[1:], dtype=src.dtype)
    return out.index_add_(0, seg, src)
def _pad(x, pw):
    flat = tuple(v for pair in reversed(pw) for v in pair)
    return torch.nn.functional.pad(x, flat)

kernel_name = 'concat_heads__grouped_kv_mqa__ssm_conv_h3__prompt'

def setup_inputs():

    _w = (lambda a: (lambda t: t.to(torch.bfloat16) if t.is_floating_point() else t)(torch.as_tensor(a)))
    def _A_setup():
        B, S, D, H = 1, 2048, 512, 32
        Dh = D // H
        return {
            "x": _w((lambda *s: torch.randn(*s, dtype=torch.bfloat16) * 0.05)(B, S, D)),
            "wq": _w((lambda *s: torch.randn(*s, dtype=torch.bfloat16) * 0.8)(D, H * Dh)),
            "wk": _w((lambda *s: torch.randn(*s, dtype=torch.bfloat16) * 0.8)(D, Dh)),
            "wv": _w((lambda *s: torch.randn(*s, dtype=torch.bfloat16) * 0.05)(D, Dh)),
            "wo": _w((lambda *s: torch.randn(*s, dtype=torch.bfloat16) * 0.05)(H * Dh, D)),
        }
    _A_in = _A_setup()

    def _B_setup():
        B, S, D = 1, 2048, 512
        ks = 4
        return {
            "q": _w((lambda *s: torch.randn(*s, dtype=torch.bfloat16) * 0.05)(B, S, D)),
            "k": _w((lambda *s: torch.randn(*s, dtype=torch.bfloat16) * 0.2)(B, S, D)),
            "v": _w((lambda *s: torch.randn(*s, dtype=torch.bfloat16) * 0.2)(B, S, D)),
            "shift_k": _w((lambda *s: torch.randn(*s, dtype=torch.bfloat16) * 0.2)(D, ks) * 0.5),
            "ssm_k": _w((lambda *s: torch.randn(*s, dtype=torch.bfloat16) * 0.4)(D, S) * (1.0 / S ** 0.5)),
            "ssm_D": _w((lambda *s: torch.randn(*s, dtype=torch.bfloat16) * 0.2)(D)),
        }
    _B_in = _B_setup()

    return {
        "A_x": _A_in["x"],
        "A_wq": _A_in["wq"],
        "A_wk": _A_in["wk"],
        "A_wv": _A_in["wv"],
        "A_wo": _A_in["wo"],
        "B_q": _B_in["q"],
        "B_k": _B_in["k"],
        "B_v": _B_in["v"],
        "B_shift_k": _B_in["shift_k"],
        "B_ssm_k": _B_in["ssm_k"],
        "B_ssm_D": _B_in["ssm_D"],
    }

def reference(A_x, A_wq, A_wk, A_wv, A_wo, B_q, B_k, B_v, B_shift_k, B_ssm_k, B_ssm_D):
    def _A_ref(x, wq, wk, wv, wo):
        B, S, D = x.shape
        H = 32
        Dh = D // H
        q = (lambda x, *d: x.permute(*d))((x @ wq).reshape(B, S, H, Dh), 0, 2, 1, 3)
        k = x @ wk
        v = x @ wv
        scores = torch.einsum("bhqd,bkd->bhqk", q, k) * (Dh ** -0.5)
        p = _softmax(scores, axis=-1)
        o = torch.einsum("bhqk,bkd->bhqd", p, v)
        return (lambda x, *d: x.permute(*d))(o, 0, 2, 1, 3).reshape(B, S, H * Dh) @ wo

    def _B_ref(q, k, v, shift_k, ssm_k, ssm_D):
        B, S, D = q.shape
        ks = shift_k.shape[-1]
        k_pad = _pad(k, ((0, 0), (ks - 1, 0), (0, 0)))
        k_shift = torch.zeros_like(k)
        for t in range(ks):
            k_shift = k_shift + k_pad[:, t:t + S, :] * shift_k[:, ks - 1 - t][None, None, :]
        kv = k_shift * v
        L = 2 * S
        kv_pad = _pad(kv, ((0, 0), (0, S), (0, 0)))
        kf_pad = _pad(ssm_k, ((0, 0), (0, S)))
        kv_f = (lambda x, axes=(-2,-1): torch.fft.rfft2(x.float(), dim=axes))(kv_pad[:, None, :, :], axes=(1, 2))
        kf_f = (lambda x, axes=(-2,-1): torch.fft.rfft2(x.float(), dim=axes))(kf_pad[:, None, :], axes=(1, 2))
        kf_f = (lambda x, *d: x.permute(*d))(kf_f, 1, 2, 0)
        y_f = kv_f * kf_f[None, :, :, :]
        y = (lambda x, s, axes=(-2,-1): torch.fft.irfft2(x, s=s, dim=axes).to(torch.bfloat16))(y_f, (1, L), axes=(1, 2))[:, 0, :S, :]
        y = y + kv * ssm_D[None, None, :]
        return q * y

    a = _A_ref(A_x, A_wq, A_wk, A_wv, A_wo)
    b = _B_ref(B_q, B_k, B_v, B_shift_k, B_ssm_k, B_ssm_D)
    return torch.concatenate([a, b], axis=-1)
```

```cpp
#include <hip/hip_runtime.h>
#include <hip/hip_bf16.h>

typedef __attribute__((ext_vector_type(8))) __bf16 bf16x8;
typedef __attribute__((ext_vector_type(2))) __bf16 bf16x2;
typedef __attribute__((ext_vector_type(16))) float f32x16;
typedef __attribute__((ext_vector_type(4))) float f32x4;
typedef __attribute__((ext_vector_type(2))) float f32x2;
typedef __attribute__((ext_vector_type(4))) unsigned u32x4;
typedef __attribute__((ext_vector_type(2))) unsigned u32x2;
typedef unsigned short u16;

constexpr int SEQ = 2048;
constexpr int DM = 512;

constexpr size_t WS_QF = 0;
constexpr size_t WS_OF = WS_QF + 2u * 1024 * 1024;
constexpr size_t WS_ZT = WS_OF + 2u * 1024 * 1024;
constexpr size_t WS_WO = WS_ZT + 2u * 1024 * 1024;
constexpr size_t WS_KV = WS_WO + 512u * 1024;
constexpr int K_BYTES = 64 * 1024;
constexpr int V_BLK = 528;
constexpr int KV_BYTES = K_BYTES + 128 * V_BLK;
constexpr size_t WS_KMAX = WS_KV + 135168;
constexpr size_t WS_UR = WS_KMAX + 1024;
constexpr int UR_RS = 2080;
constexpr int UR_PAD = 8;

constexpr float QSCALE = 0.25f * 1.4426950408889634f;

__device__ __forceinline__ float bf2f(u16 v) { return __uint_as_float(((unsigned)v) << 16); }
__device__ __forceinline__ float bflo(unsigned v) { return __uint_as_float(v << 16); }
__device__ __forceinline__ float bfhi(unsigned v) { return __uint_as_float(v & 0xffff0000u); }
__device__ __forceinline__ unsigned pack2(float lo, float hi) {
    f32x2 f = {lo, hi};
    bf16x2 b = __builtin_convertvector(f, bf16x2);
    return __builtin_bit_cast(unsigned, b);
}
__device__ __forceinline__ float rbf(float x) { return (float)(__bf16)x; }
__device__ __forceinline__ bf16x8 as_bf(u32x4 v) { return __builtin_bit_cast(bf16x8, v); }
__device__ __forceinline__ f32x16 mfma32(u32x4 a, u32x4 b, f32x16 c) {
    return __builtin_amdgcn_mfma_f32_32x32x16_bf16(as_bf(a), as_bf(b), c, 0, 0, 0);
}
__device__ __forceinline__ f32x4 mfma16(u32x4 a, u32x4 b, f32x4 c) {
    return __builtin_amdgcn_mfma_f32_16x16x32_bf16(as_bf(a), as_bf(b), c, 0, 0, 0);
}
__device__ __forceinline__ u32x4 ld16(const void* p) { return *reinterpret_cast<const u32x4*>(p); }
__device__ __forceinline__ u32x2 ld8(const void* p) { return *reinterpret_cast<const u32x2*>(p); }
__device__ __forceinline__ float xor32(float v) { return __shfl_xor(v, 32); }

__device__ __forceinline__ void decode_block(int b, int& tg, int& hs) {
    const int r = b >> 3;
    tg = (b & 7) * 4 + (r >> 3);
    hs = r & 7;
}

__device__ __forceinline__ void uprep(const u16* __restrict__ bk, const u16* __restrict__ bv,
                                      const u16* __restrict__ shk, u16* __restrict__ ur,
                                      int tg, int cg, int wv, int lane) {
    const int c8 = lane & 7;
    const int st = (lane >> 3) + 8 * wv;
    const int s0 = 64 * tg + 4 * st;
    const int d0 = 64 * cg + 8 * c8;
    u32x4 kr[7], vr[4], sk[4];
#pragma unroll
    for (int i = 0; i < 7; ++i) {
        const int s = s0 - 3 + i;
        kr[i] = (u32x4){0, 0, 0, 0};
        if (s >= 0) kr[i] = ld16(bk + (size_t)s * DM + d0);
    }
#pragma unroll
    for (int i = 0; i < 4; ++i) vr[i] = ld16(bv + (size_t)(s0 + i) * DM + d0);
#pragma unroll
    for (int i = 0; i < 4; ++i) sk[i] = ld16(shk + (size_t)d0 * 4 + 8 * i);
    const int n0 = (SEQ - 4) - s0;
#pragma unroll
    for (int e = 0; e < 8; ++e) {
        const unsigned w01 = sk[e >> 1][(e & 1) * 2], w23 = sk[e >> 1][(e & 1) * 2 + 1];
        const float w0 = bflo(w01), w1 = bfhi(w01), w2 = bflo(w23), w3 = bfhi(w23);
        float kk[7], u[4];
#pragma unroll
        for (int i = 0; i < 7; ++i) kk[i] = (e & 1) ? bfhi(kr[i][e >> 1]) : bflo(kr[i][e >> 1]);
#pragma unroll
        for (int i = 0; i < 4; ++i) {
            float a = rbf(kk[i] * w3);
            a = rbf(a + rbf(kk[i + 1] * w2));
            a = rbf(a + rbf(kk[i + 2] * w1));
            a = rbf(a + rbf(kk[i + 3] * w0));
            const float vv = (e & 1) ? bfhi(vr[i][e >> 1]) : bflo(vr[i][e >> 1]);
            u[i] = a * vv;
        }
        const u32x2 o = {pack2(u[3], u[2]), pack2(u[1], u[0])};
        u16* r0 = ur + (size_t)(d0 + e) * 2 * UR_RS + UR_PAD;
        u16* r1 = r0 + UR_RS;
        *reinterpret_cast<u32x2*>(r0 + n0) = o;
        r1[n0 - 1] = (u16)(o[0] & 0xffff);
        *reinterpret_cast<unsigned*>(r1 + n0) = (o[0] >> 16) | (o[1] << 16);
        r1[n0 + 2] = (u16)(o[1] >> 16);
        if (s0 == 0) {
            const u32x4 z = {0, 0, 0, 0};
            *reinterpret_cast<u32x4*>(r0 + SEQ) = z;
            *reinterpret_cast<u32x4*>(r0 + SEQ + 8) = z;
            r1[SEQ - 1] = 0;
            *reinterpret_cast<u32x4*>(r1 + SEQ) = z;
            *reinterpret_cast<u32x4*>(r1 + SEQ + 8) = z;
        }
    }
}

__global__ void __launch_bounds__(768)
prep_kernel(const u16* __restrict__ x, const u16* __restrict__ wq, const u16* __restrict__ wk,
            const u16* __restrict__ wv, const u16* __restrict__ wo, const u16* __restrict__ bk,
            const u16* __restrict__ bv, const u16* __restrict__ shk, char* __restrict__ ws) {
    __shared__ __attribute__((aligned(16))) float red[(64 + 24) * 256];
    const int lane = threadIdx.x & 63;
    const int w = __builtin_amdgcn_readfirstlane(threadIdx.x >> 6);
    int tg, hs;
    decode_block(blockIdx.x, tg, hs);
    const int r = lane & 31, h = lane >> 5;

    if (w < 8) {
        const int i = w >> 2, kq = w & 3;
        const u16* xp = x + (size_t)(64 * tg + 32 * i + r) * DM + 128 * kq + 8 * h;
        const u16* wp = wq + (size_t)(128 * kq + 8 * h) * DM + 64 * hs + 2 * r;
        u32x4 xa[8];
        unsigned wd[8][8];
#pragma unroll
        for (int s = 0; s < 8; ++s) {
            xa[s] = ld16(xp + 16 * s);
#pragma unroll
            for (int j = 0; j < 8; ++j)
                wd[s][j] = *reinterpret_cast<const unsigned*>(wp + (size_t)(16 * s + j) * DM);
        }
        f32x16 ae = {0}, ao = {0};
#pragma unroll
        for (int s = 0; s < 8; ++s) {
            u32x4 fe, fo;
#pragma unroll
            for (int j = 0; j < 4; ++j) {
                fe[j] = __builtin_amdgcn_perm(wd[s][2 * j + 1], wd[s][2 * j], 0x05040100u);
                fo[j] = __builtin_amdgcn_perm(wd[s][2 * j + 1], wd[s][2 * j], 0x07060302u);
            }
            ae = mfma32(fe, xa[s], ae);
            ao = mfma32(fo, xa[s], ao);
        }
        f32x4* rp = reinterpret_cast<f32x4*>(red) + w * 512 + lane;
#pragma unroll
        for (int hd = 0; hd < 4; ++hd) {
            rp[(2 * hd) * 64] = (f32x4){ae[4 * hd], ae[4 * hd + 1], ae[4 * hd + 2], ae[4 * hd + 3]};
            rp[(2 * hd + 1) * 64] = (f32x4){ao[4 * hd], ao[4 * hd + 1], ao[4 * hd + 2], ao[4 * hd + 3]};
        }
    } else if (hs < 2) {
        const int kq = w - 8;
        const int tt = 2 * tg + hs;
        const u16* xp = x + (size_t)(32 * tt + r) * DM + 128 * kq + 8 * h;
        const int c = r & 15;
        const int col = (r < 16) ? (((c >> 2) & 1) * 8 + (c >> 3) * 4 + (c & 3)) : c;
        const u16* wp = ((r < 16) ? wk : wv) + (size_t)(128 * kq + 8 * h) * 16 + col;
        u32x4 xa[8];
        u16 wd[8][8];
#pragma unroll
        for (int s = 0; s < 8; ++s) {
            xa[s] = ld16(xp + 16 * s);
#pragma unroll
            for (int j = 0; j < 8; ++j) wd[s][j] = wp[(16 * s + j) * 16];
        }
        f32x16 a1 = {0}, a2 = {0};
#pragma unroll
        for (int s = 0; s < 8; ++s) {
            u32x4 f;
#pragma unroll
            for (int j = 0; j < 4; ++j) f[j] = (unsigned)wd[s][2 * j] | ((unsigned)wd[s][2 * j + 1] << 16);
            a1 = mfma32(f, xa[s], a1);
            a2 = mfma32(xa[s], f, a2);
        }
        f32x4* rp = reinterpret_cast<f32x4*>(red) + 8 * 512 + kq * 384 + lane;
#pragma unroll
        for (int g = 0; g < 2; ++g)
            rp[g * 64] = (f32x4){a1[4 * g], a1[4 * g + 1], a1[4 * g + 2], a1[4 * g + 3]};
#pragma unroll
        for (int g = 0; g < 4; ++g)
            rp[(2 + g) * 64] = (f32x4){a2[4 * g], a2[4 * g + 1], a2[4 * g + 2], a2[4 * g + 3]};
    } else if (w < 10) {
        uprep(bk, bv, shk, reinterpret_cast<u16*>(ws + WS_UR), tg, hs, w - 8, lane);
    } else if (hs < 4) {
        uprep(bk, bv, shk, reinterpret_cast<u16*>(ws + WS_UR), tg, hs - 2, w - 10, lane);
    } else {
        const int sig = 16 * ((r >> 2) & 1) + (r & 3) + 4 * (r >> 3);
#pragma unroll
        for (int q = 0; q < 2; ++q) {
            const int f = ((tg * 4 + (hs - 4)) * 2 + (w - 10)) * 2 + q;
            const int hd = f >> 4, ct = f & 15;
            const u16* wp = wo + (size_t)(16 * hd + 4 * h) * DM + 32 * ct + sig;
            u16 e[8];
#pragma unroll
            for (int j = 0; j < 8; ++j) e[j] = wp[(size_t)((j & 3) + 8 * (j >> 2)) * DM];
            u32x4 o;
#pragma unroll
            for (int j = 0; j < 4; ++j) o[j] = (unsigned)e[2 * j] | ((unsigned)e[2 * j + 1] << 16);
            *reinterpret_cast<u32x4*>(ws + WS_WO + (size_t)f * 1024 + lane * 16) = o;
        }
    }
    __syncthreads();
    if (w < 8) {
        const int i = w >> 2, hd = w & 3;
        const f32x4* rp = reinterpret_cast<const f32x4*>(red) + (4 * i) * 512 + (2 * hd) * 64 + lane;
        f32x4 e = rp[0], o = rp[64];
#pragma unroll
        for (int q = 1; q < 4; ++q) {
            e += rp[q * 512];
            o += rp[q * 512 + 64];
        }
        u32x4 qf;
#pragma unroll
        for (int j = 0; j < 4; ++j) qf[j] = pack2(e[j] * QSCALE, o[j] * QSCALE);
        *reinterpret_cast<u32x4*>(ws + WS_QF + ((size_t)(2 * tg + i) * 32 + 4 * hs + hd) * 1024 + lane * 16) = qf;
    } else if (hs < 2 && w < 11) {
        const int tt = 2 * tg + hs;
        const int part = w - 8;
        const f32x4* rp = reinterpret_cast<const f32x4*>(red) + 8 * 512 + (2 * part) * 64 + lane;
        f32x4 a = rp[0], b = rp[64];
#pragma unroll
        for (int q = 1; q < 4; ++q) {
            a += rp[q * 384];
            b += rp[q * 384 + 64];
        }
        const u32x4 f = {pack2(a[0], a[1]), pack2(a[2], a[3]), pack2(b[0], b[1]), pack2(b[2], b[3])};
        char* kv = ws + WS_KV;
        if (part == 0) {
            *reinterpret_cast<u32x4*>(kv + tt * 1024 + lane * 16) = f;
            float n2 = 0.f;
#pragma unroll
            for (int j = 0; j < 4; ++j) n2 += bflo(f[j]) * bflo(f[j]) + bfhi(f[j]) * bfhi(f[j]);
            n2 += xor32(n2);
#pragma unroll
            for (int m = 16; m >= 1; m >>= 1) n2 = fmaxf(n2, __shfl_xor(n2, m));
            if (lane == 0) reinterpret_cast<float*>(ws + WS_KMAX)[tt] = n2;
        } else {
            char* blk = kv + K_BYTES + (2 * tt + part - 1) * V_BLK;
            if (r >= 16) *reinterpret_cast<u32x4*>(blk + h * 256 + (r - 16) * 16) = f;
            if (lane == 0) {
                const u32x4 ones = {0x3f803f80u, 0x3f803f80u, 0x3f803f80u, 0x3f803f80u};
                *reinterpret_cast<u32x4*>(blk + 512) = ones;
            }
        }
    }
}

__device__ __forceinline__ u32x4 lds16(const char* p) { return *reinterpret_cast<const u32x4*>(p); }

__device__ __forceinline__ f32x16 attn_pass(const char* kb, const char* vb, u32x4 q, float negm) {
    f32x16 c;
#pragma unroll
    for (int j = 0; j < 16; ++j) c[j] = negm;
    f32x16 o = {0};
    f32x16 s = mfma32(lds16(kb), q, c);
    for (int t0 = 0; t0 < 64; t0 += 8) {
#pragma unroll
        for (int u = 0; u < 8; ++u) {
            const int tn = (u == 7) ? ((t0 + 8) & 63) : (t0 + u + 1);
            const u32x4 kf = lds16(kb + tn * 1024);
            const u32x4 v0 = lds16(vb + (t0 + u) * 2 * V_BLK);
            const u32x4 v1 = lds16(vb + (t0 + u) * 2 * V_BLK + V_BLK);
            const f32x16 sn = mfma32(kf, q, c);
            u32x4 p0, p1;
#pragma unroll
            for (int j = 0; j < 4; ++j) {
                p0[j] = pack2(__builtin_amdgcn_exp2f(s[2 * j]), __builtin_amdgcn_exp2f(s[2 * j + 1]));
                p1[j] = pack2(__builtin_amdgcn_exp2f(s[8 + 2 * j]), __builtin_amdgcn_exp2f(s[8 + 2 * j + 1]));
            }
            o = mfma32(v0, p0, o);
            o = mfma32(v1, p1, o);
            s = sn;
        }
    }
    return o;
}

__device__ __forceinline__ float row_max(const char* kb, u32x4 q) {
    float m = -__builtin_inff();
    const f32x16 z = {0};
    for (int t = 0; t < 64; ++t) {
        const f32x16 s = mfma32(lds16(kb + t * 1024), q, z);
#pragma unroll
        for (int j = 0; j < 16; ++j) m = fmaxf(m, s[j]);
    }
    return fmaxf(m, xor32(m));
}

template <int T0, int T1, int T2, int T3>
__device__ __forceinline__ void conv_wave(const u16* __restrict__ hk, const u16* __restrict__ ur,
                                          u16* __restrict__ zt, float dd, int lane) {
    constexpr int NF = T3 + 1;
    const int r = lane & 15, g = lane >> 4;
    const int ns = (SEQ - 1) - 16 * (g >> 1) - r + 8 * (g & 1);
    const u16* ap = ur + UR_PAD + ((ns & 1) ? (UR_RS + ns - 1) : ns);
    const int boff = 16 * r - 16 * (g >> 1) + 8 * (g & 1);
    const u16* bp = hk + boff;
    f32x4 c0 = {0}, c1 = {0}, c2 = {0}, c3 = {0};
    for (int rho = 0; rho < 8; ++rho) {
        u32x4 A[NF], B[NF];
#pragma unroll
        for (int a = 0; a < NF; ++a) A[a] = ld16(ap - 32 * rho - 256 * a);
        B[0] = (u32x4){0, 0, 0, 0};
        if (boff - 32 * rho >= 0) B[0] = ld16(bp - 32 * rho);
#pragma unroll
        for (int b = 1; b < NF; ++b) B[b] = ld16(bp - 32 * rho + 256 * b);
#pragma unroll
        for (int a = 0; a < NF; ++a) {
            if (a <= T3) c3 = mfma16(A[a], B[T3 - a], c3);
            if (a <= T2) c2 = mfma16(A[a], B[a <= T2 ? T2 - a : 0], c2);
            if (a <= T1) c1 = mfma16(A[a], B[a <= T1 ? T1 - a : 0], c1);
            if (a <= T0) c0 = mfma16(A[a], B[a <= T0 ? T0 - a : 0], c0);
        }
    }
    const int ts[4] = {T0, T1, T2, T3};
    const f32x4 cs[4] = {c0, c1, c2, c3};
#pragma unroll
    for (int i = 0; i < 4; ++i) {
        const int s = 256 * ts[i] + 16 * r + 4 * g;
        const u32x2 uu = ld8(ur + UR_PAD + (SEQ - 4) - s);
        const float u0 = bfhi(uu[1]), u1 = bflo(uu[1]), u2 = bfhi(uu[0]), u3 = bflo(uu[0]);
        const u32x2 o = {pack2(rbf(cs[i][0]) + rbf(u0 * dd), rbf(cs[i][1]) + rbf(u1 * dd)),
                         pack2(rbf(cs[i][2]) + rbf(u2 * dd), rbf(cs[i][3]) + rbf(u3 * dd))};
        *reinterpret_cast<u32x2*>(zt + s) = o;
    }
}

__global__ void __launch_bounds__(768)
main_kernel(const u16* __restrict__ ssmk, const u16* __restrict__ ssmd, char* __restrict__ ws) {
    extern __shared__ __attribute__((aligned(16))) char smem[];
    const int lane = threadIdx.x & 63;
    const int w = __builtin_amdgcn_readfirstlane(threadIdx.x >> 6);
    int tg, hs;
    decode_block(blockIdx.x, tg, hs);

    const char* img = ws + WS_KV;
    for (int c = w; c < KV_BYTES / 1024; c += 12)
        __builtin_amdgcn_global_load_lds(img + c * 1024 + lane * 16,
                                         (__attribute__((address_space(3))) void*)(smem + c * 1024), 16, 0, 0);
    if (w < 8) {
        const size_t fo = ((size_t)(2 * tg + (w >> 2)) * 32 + 4 * hs + (w & 3)) * 1024 + lane * 16;
        const u32x4 q = ld16(ws + WS_QF + fo);
        float km = reinterpret_cast<const float*>(ws + WS_KMAX)[lane];
#pragma unroll
        for (int m = 32; m >= 1; m >>= 1) km = fmaxf(km, __shfl_xor(km, m));
        float n2 = 0.f;
#pragma unroll
        for (int j = 0; j < 4; ++j) n2 += bflo(q[j]) * bflo(q[j]) + bfhi(q[j]) * bfhi(q[j]);
        n2 += xor32(n2);
        float m = sqrtf(n2 * km);
        const int r = lane & 31, h = lane >> 5;
        const int voff = (r < 16) ? (h * 256 + r * 16)
                                  : (r == 16 ? 512 : (h * 256 + ((r >= 20 && r < 28) ? 0 : 64)));
        const char* kb = smem + lane * 16;
        const char* vb = smem + K_BYTES + voff;
        __syncthreads();
        f32x16 o = attn_pass(kb, vb, q, -m);
        float l = __shfl(o[8], r);
        if (__any(!(l >= 5.9604645e-8f && l <= 3.0e38f))) {
            m = row_max(kb, q);
            o = attn_pass(kb, vb, q, -m);
            l = __shfl(o[8], r);
        }
        const float inv = 1.0f / l;
        u32x4 of;
#pragma unroll
        for (int j = 0; j < 4; ++j) of[j] = pack2(o[2 * j] * inv, o[2 * j + 1] * inv);
        *reinterpret_cast<u32x4*>(ws + WS_OF + fo) = of;
    } else {
        __syncthreads();
        const int d = 2 * blockIdx.x + ((w - 8) >> 1);
        const u16* hk = ssmk + (size_t)d * SEQ;
        const u16* ur = reinterpret_cast<const u16*>(ws + WS_UR) + (size_t)d * 2 * UR_RS;
        u16* zt = reinterpret_cast<u16*>(ws + WS_ZT) + (size_t)d * SEQ;
        const float dd = bf2f(ssmd[d]);
        if (w & 1) conv_wave<1, 2, 4, 7>(hk, ur, zt, dd, lane);
        else conv_wave<0, 3, 5, 6>(hk, ur, zt, dd, lane);
    }
}

__global__ void __launch_bounds__(384)
out_kernel(const u16* __restrict__ bq, const char* __restrict__ ws, u16* __restrict__ out) {
    __shared__ __attribute__((aligned(16))) float red[64 * 256];
    const int lane = threadIdx.x & 63;
    const int w = __builtin_amdgcn_readfirstlane(threadIdx.x >> 6);
    int tg, hs;
    decode_block(blockIdx.x, tg, hs);
    if (w < 4) {
        const char* op = ws + WS_OF + ((size_t)(2 * tg) * 32 + 8 * w) * 1024 + lane * 16;
        const char* wp = ws + WS_WO + ((size_t)(8 * w) * 16 + 2 * hs) * 1024 + lane * 16;
        u32x4 of[2][8], wf[2][8];
#pragma unroll
        for (int s = 0; s < 8; ++s) {
            of[0][s] = ld16(op + s * 1024);
            of[1][s] = ld16(op + (32 + s) * 1024);
            wf[0][s] = ld16(wp + s * 16 * 1024);
            wf[1][s] = ld16(wp + (s * 16 + 1) * 1024);
        }
        f32x16 acc[2][2] = {{{0}, {0}}, {{0}, {0}}};
#pragma unroll
        for (int s = 0; s < 8; ++s)
#pragma unroll
            for (int i = 0; i < 2; ++i)
#pragma unroll
                for (int c = 0; c < 2; ++c) acc[i][c] = mfma32(wf[c][s], of[i][s], acc[i][c]);
        f32x4* rp = reinterpret_cast<f32x4*>(red) + w * 1024 + lane;
#pragma unroll
        for (int i = 0; i < 2; ++i)
#pragma unroll
            for (int c = 0; c < 2; ++c)
#pragma unroll
                for (int g = 0; g < 4; ++g)
                    rp[((i * 2 + c) * 4 + g) * 64] = (f32x4){acc[i][c][4 * g], acc[i][c][4 * g + 1],
                                                             acc[i][c][4 * g + 2], acc[i][c][4 * g + 3]};
    } else {
        const int c8 = lane & 7;
        const int st = (lane >> 3) + 8 * (w - 4);
        const int s0 = 64 * tg + 4 * st;
        const int d0 = 64 * hs + 8 * c8;
        const u16* zt = reinterpret_cast<const u16*>(ws + WS_ZT);
        u32x2 z[8];
        u32x4 qr[4];
#pragma unroll
        for (int e = 0; e < 8; ++e) z[e] = ld8(zt + (size_t)(d0 + e) * SEQ + s0);
#pragma unroll
        for (int i = 0; i < 4; ++i) qr[i] = ld16(bq + (size_t)(s0 + i) * DM + d0);
#pragma unroll
        for (int i = 0; i < 4; ++i) {
            u32x4 o;
#pragma unroll
            for (int j = 0; j < 4; ++j) {
                const unsigned za = z[2 * j][i >> 1], zb = z[2 * j + 1][i >> 1];
                const float a = (i & 1) ? bfhi(za) : bflo(za);
                const float b = (i & 1) ? bfhi(zb) : bflo(zb);
                o[j] = pack2(bflo(qr[i][j]) * a, bfhi(qr[i][j]) * b);
            }
            *reinterpret_cast<u32x4*>(out + (size_t)(s0 + i) * 1024 + 512 + d0) = o;
        }
    }
    __syncthreads();
    if (w < 4) {
        const f32x4* rp = reinterpret_cast<const f32x4*>(red) + w * 256 + lane;
        f32x4 a[4];
#pragma unroll
        for (int g = 0; g < 4; ++g) a[g] = rp[g * 64];
#pragma unroll
        for (int q = 1; q < 4; ++q)
#pragma unroll
            for (int g = 0; g < 4; ++g) a[g] += rp[q * 1024 + g * 64];
        const int tok = 64 * tg + 32 * (w >> 1) + (lane & 31);
        u16* dst = out + (size_t)tok * 1024 + 64 * hs + 32 * (w & 1) + 16 * (lane >> 5);
        const u32x4 o0 = {pack2(a[0][0], a[0][1]), pack2(a[0][2], a[0][3]), pack2(a[1][0], a[1][1]), pack2(a[1][2], a[1][3])};
        const u32x4 o1 = {pack2(a[2][0], a[2][1]), pack2(a[2][2], a[2][3]), pack2(a[3][0], a[3][1]), pack2(a[3][2], a[3][3])};
        *reinterpret_cast<u32x4*>(dst) = o0;
        *reinterpret_cast<u32x4*>(dst + 8) = o1;
    }
}

extern "C" void kernel_launch(void* const* d_in, const int* in_sizes, int n_in, void* d_out,
                              int out_size, void* d_ws, size_t ws_size, hipStream_t stream) {
    const u16* x = (const u16*)d_in[0];
    const u16* wq = (const u16*)d_in[1];
    const u16* wk = (const u16*)d_in[2];
    const u16* wv = (const u16*)d_in[3];
    const u16* wo = (const u16*)d_in[4];
    const u16* bq = (const u16*)d_in[5];
    const u16* bk = (const u16*)d_in[6];
    const u16* bv = (const u16*)d_in[7];
    const u16* shk = (const u16*)d_in[8];
    const u16* ssmk = (const u16*)d_in[9];
    const u16* ssmd = (const u16*)d_in[10];
    char* ws = (char*)d_ws;
    (void)hipFuncSetAttribute((const void*)main_kernel, hipFuncAttributeMaxDynamicSharedMemorySize, KV_BYTES);
    prep_kernel<<<256, 768, 0, stream>>>(x, wq, wk, wv, wo, bk, bv, shk, ws);
    main_kernel<<<256, 768, KV_BYTES, stream>>>(ssmk, ssmd, ws);
    out_kernel<<<256, 384, 0, stream>>>(bq, ws, (u16*)d_out);
}
```

```cpp
#include <hip/hip_runtime.h>
#include <hip/hip_bf16.h>

typedef __attribute__((ext_vector_type(8))) __bf16 bf16x8;
typedef __attribute__((ext_vector_type(2))) __bf16 bf16x2;
typedef __attribute__((ext_vector_type(16))) float f32x16;
typedef __attribute__((ext_vector_type(4))) float f32x4;
typedef __attribute__((ext_vector_type(2))) float f32x2;
typedef __attribute__((ext_vector_type(4))) unsigned u32x4;
typedef __attribute__((ext_vector_type(2))) unsigned u32x2;
typedef __attribute__((ext_vector_type(4))) short s16x4;
typedef unsigned long long u64;
typedef unsigned short u16;
#define LDSP __attribute__((address_space(3)))
#define GLBP __attribute__((address_space(1)))

constexpr int SEQ = 2048;
constexpr int DM = 512;

constexpr int WS_EPOCH = 0;
constexpr int WS_KVF = 1024;
constexpr int WS_URF = 4096;
constexpr int WS_ZF = 8192;
constexpr int WS_OFL = 16384;
constexpr int WS_KV = 65536;
constexpr int WS_UR = WS_KV + 131072;
constexpr int WS_ZT = WS_UR + 2097152 + 4096;
constexpr int WS_OF = WS_ZT + 2097152;
constexpr int WS_END = WS_OF + 2097152;

constexpr int L_X = 0;
constexpr int L_W = 65536;
constexpr int L_WK = 131072;
constexpr int L_WV = 147456;
constexpr int L_K = 0;
constexpr int L_V = 65536;
constexpr int L_CONST = 131072;
constexpr int L_CNT = L_CONST + 9 * 512;
constexpr int L_U = L_CNT + 64;
constexpr int U_RS = 4160;
constexpr int LDS_BYTES = 163840;

constexpr float QSCALE = 0.25f * 1.4426950408889634f;
constexpr unsigned SPIN_CAP = 1u << 18;

__device__ __forceinline__ float bf2f(u16 v) { return __uint_as_float(((unsigned)v) << 16); }
__device__ __forceinline__ float bflo(unsigned v) { return __uint_as_float(v << 16); }
__device__ __forceinline__ float bfhi(unsigned v) { return __uint_as_float(v & 0xffff0000u); }
__device__ __forceinline__ unsigned pack2(float lo, float hi) {
    f32x2 f = {lo, hi};
    bf16x2 b = __builtin_convertvector(f, bf16x2);
    return __builtin_bit_cast(unsigned, b);
}
__device__ __forceinline__ float rbf(float x) { return (float)(__bf16)x; }
__device__ __forceinline__ bf16x8 as_bf(u32x4 v) { return __builtin_bit_cast(bf16x8, v); }
__device__ __forceinline__ f32x16 mfma32(u32x4 a, u32x4 b, f32x16 c) {
    return __builtin_amdgcn_mfma_f32_32x32x16_bf16(as_bf(a), as_bf(b), c, 0, 0, 0);
}
__device__ __forceinline__ f32x4 mfma16(u32x4 a, u32x4 b, f32x4 c) {
    return __builtin_amdgcn_mfma_f32_16x16x32_bf16(as_bf(a), as_bf(b), c, 0, 0, 0);
}
__device__ __forceinline__ u32x4 ld16(const void* p) { return *reinterpret_cast<const u32x4*>(p); }
__device__ __forceinline__ u32x2 ld8(const void* p) { return *reinterpret_cast<const u32x2*>(p); }
__device__ __forceinline__ u32x4 tr_frag(const char* p, int second) {
    const s16x4 a = __builtin_amdgcn_ds_read_tr16_b64_v4i16((LDSP s16x4*)p);
    const s16x4 b = __builtin_amdgcn_ds_read_tr16_b64_v4i16((LDSP s16x4*)(p + second));
    const u32x2 a2 = __builtin_bit_cast(u32x2, a), b2 = __builtin_bit_cast(u32x2, b);
    return (u32x4){a2[0], a2[1], b2[0], b2[1]};
}
__device__ __forceinline__ void dma16(const void* src, char* dst) {
    __builtin_amdgcn_global_load_lds(src, (LDSP void*)dst, 16, 0, 0);
}
__device__ __forceinline__ void dma16_dev(const void* src, char* dst) {
    __builtin_amdgcn_global_load_lds(src, (LDSP void*)dst, 16, 0, 16);
}
__device__ __forceinline__ void barrier() {
    asm volatile("s_waitcnt lgkmcnt(0)\n\ts_barrier" ::: "memory");
}
__device__ __forceinline__ void drain() { asm volatile("s_waitcnt vmcnt(0)" ::: "memory"); }

__device__ __forceinline__ void publish(char* ws, int off, u64 tag, int lane) {
    drain();
    if (lane == 0) __hip_atomic_store((GLBP u64*)(ws + off), tag, __ATOMIC_RELAXED, __HIP_MEMORY_SCOPE_AGENT);
}
__device__ __forceinline__ void wait_flags(char* ws, int off, bool active, u64 tag) {
#pragma unroll 1
    for (unsigned spin = 0; spin < SPIN_CAP; ++spin) {
        u64 v = tag;
        if (active) v = __hip_atomic_load((GLBP u64*)(ws + off), __ATOMIC_RELAXED, __HIP_MEMORY_SCOPE_AGENT);
        if (__all(v == tag)) break;
        __builtin_amdgcn_s_sleep(2);
    }
    asm volatile("" ::: "memory");
}

#define STR2(x) #x
#define STR(x) STR2(x)
#define EX(r) "v_exp_f32 v" STR(r) ", v" STR(r) "\n"
#define CV(d, a, b) "v_cvt_pk_bf16_f32 v" STR(d) ", v" STR(a) ", v" STR(b) "\n"
#define TILE(x0, x1, x2, x3, x4, x5, x6, x7, x8, x9, x10, x11, x12, x13, x14, x15, NEXT, u)     \
    "s_waitcnt lgkmcnt(2)\n"                                                                    \
    "v_mfma_f32_32x32x16_bf16 " NEXT ", v[104:107], %[q], v[116:131]\n"                         \
    "ds_read_b128 v[104:107], %[ka] offset:" STR((u + 2) * 1024) "\n"                           \
    EX(x0) EX(x1) EX(x2) EX(x3) CV(96, x0, x1) EX(x4) EX(x5) CV(97, x2, x3)                     \
    "s_waitcnt lgkmcnt(2)\n"                                                                    \
    "v_mfma_f32_32x32x16_bf16 %[o], v[112:115], v[100:103], %[o]\n"                             \
    "ds_read_b128 v[112:115], %[va] offset:" STR((2 * u + 1) * 512) "\n"                        \
    EX(x6) EX(x7) CV(98, x4, x5) EX(x8) CV(99, x6, x7) EX(x9) EX(x10) EX(x11)                   \
    "s_waitcnt lgkmcnt(2)\n"                                                                    \
    "v_mfma_f32_32x32x16_bf16 %[o], v[108:111], v[96:99], %[o]\n"                               \
    "ds_read_b128 v[108:111], %[va] offset:" STR((2 * u + 2) * 512) "\n"                        \
    CV(100, x8, x9) EX(x12) EX(x13) CV(101, x10, x11) EX(x14) EX(x15) CV(102, x12, x13)         \
    "s_nop 0\n" CV(103, x14, x15)
#define TILE_X(u) TILE(64, 65, 66, 67, 68, 69, 70, 71, 72, 73, 74, 75, 76, 77, 78, 79, "v[80:95]", u)
#define TILE_Y(u) TILE(80, 81, 82, 83, 84, 85, 86, 87, 88, 89, 90, 91, 92, 93, 94, 95, "v[64:79]", u)
#define MOVC(r) "v_mov_b32 v" STR(r) ", v116\n"
#define LDS_RENDEZVOUS(off, lbl)                                                                \
    "s_mov_b64 %[sx], exec\n"                                                                   \
    "s_mov_b64 exec, 1\n"                                                                       \
    "v_mov_b32 v96, 1\n"                                                                        \
    "ds_add_u32 %[ca], v96 offset:" STR(off) "\n"                                               \
    "s_mov_b64 exec, %[sx]\n"                                                                   \
    lbl ":\n"                                                                                   \
    "ds_read_b32 v96, %[ca] offset:" STR(off) "\n"                                              \
    "s_waitcnt lgkmcnt(0)\n"                                                                    \
    "v_readfirstlane_b32 %[st], v96\n"                                                          \
    "s_cmp_lt_u32 %[st], 8\n"                                                                   \
    "s_cbranch_scc1 " lbl "b\n"
#define ATT_CLOB                                                                                \
    "v64", "v65", "v66", "v67", "v68", "v69", "v70", "v71", "v72", "v73", "v74", "v75", "v76",  \
    "v77", "v78", "v79", "v80", "v81", "v82", "v83", "v84", "v85", "v86", "v87", "v88", "v89",  \
    "v90", "v91", "v92", "v93", "v94", "v95", "v96", "v97", "v98", "v99", "v100", "v101",       \
    "v102", "v103", "v104", "v105", "v106", "v107", "v108", "v109", "v110", "v111", "v112",     \
    "v113", "v114", "v115", "v116", "v117", "v118", "v119", "v120", "v121", "v122", "v123",     \
    "v124", "v125", "v126", "v127", "v128", "v129", "v130", "v131"

__device__ __forceinline__ f32x16 attn_pass(unsigned ka, unsigned va, unsigned vstep, unsigned ca,
                                            u32x4 q, float mgiven, int given) {
    f32x16 o = {0};
    int cnt, st;
    u64 sx;
    asm volatile(
        "s_waitcnt vmcnt(14)\n"
        LDS_RENDEZVOUS(0, "10")
        "ds_read_b128 v[104:107], %[ka]\n"
        "v_mov_b32 v116, %[mg]\n"
        "s_cmp_lg_u32 %[gv], 0\n"
        "s_waitcnt lgkmcnt(0)\n"
        "s_cbranch_scc1 11f\n"
        "v_mfma_f32_32x32x16_bf16 v[64:79], v[104:107], %[q], 0\n"
        "s_nop 11\n"
        "v_max3_f32 v96, v64, v65, v66\n"
        "v_max3_f32 v97, v67, v68, v69\n"
        "v_max3_f32 v98, v70, v71, v72\n"
        "v_max3_f32 v99, v73, v74, v75\n"
        "v_max3_f32 v96, v96, v76, v77\n"
        "v_max3_f32 v97, v97, v78, v79\n"
        "v_max3_f32 v96, v96, v98, v99\n"
        "v_max_f32 v96, v96, v97\n"
        "v_mov_b32 v97, v96\n"
        "s_nop 1\n"
        "v_permlane32_swap_b32 v96, v97\n"
        "s_nop 1\n"
        "v_max_f32 v116, v96, v97\n"
        "11:\n"
        "v_xor_b32 v116, 0x80000000, v116\n"
        MOVC(117) MOVC(118) MOVC(119) MOVC(120) MOVC(121) MOVC(122) MOVC(123) MOVC(124)
        MOVC(125) MOVC(126) MOVC(127) MOVC(128) MOVC(129) MOVC(130) MOVC(131)
        "v_mov_b32 v100, 0\n v_mov_b32 v101, 0\n v_mov_b32 v102, 0\n v_mov_b32 v103, 0\n"
        "s_movk_i32 %[cnt], 16\n"
        "v_mfma_f32_32x32x16_bf16 v[64:79], v[104:107], %[q], v[116:131]\n"
        "ds_read_b128 v[104:107], %[ka] offset:1024\n"
        "ds_read_b128 v[112:115], %[va]\n"
        "ds_read_b128 v[108:111], %[va]\n"
        "s_nop 10\n"
        "12:\n"
        TILE_X(0) TILE_Y(1) TILE_X(2) TILE_Y(3)
        "v_add_u32 %[ka], 4096, %[ka]\n"
        "v_add_u32 %[va], %[vs], %[va]\n"
        "s_cmp_lg_u32 %[cnt], 16\n"
        "s_cbranch_scc1 14f\n"
        "s_waitcnt vmcnt(0)\n"
        LDS_RENDEZVOUS(4, "13")
        "14:\n"
        "s_sub_u32 %[cnt], %[cnt], 1\n"
        "s_cmp_lg_u32 %[cnt], 0\n"
        "s_cbranch_scc1 12b\n"
        "s_waitcnt lgkmcnt(0)\n"
        "v_mfma_f32_32x32x16_bf16 %[o], v[112:115], v[100:103], %[o]\n"
        "s_nop 15\n"
        : [o] "+v"(o), [ka] "+v"(ka), [va] "+v"(va), [cnt] "=&s"(cnt), [st] "=&s"(st), [sx] "=&s"(sx)
        : [q] "v"(q), [vs] "v"(vstep), [ca] "v"(ca), [mg] "v"(mgiven), [gv] "s"(given)
        : ATT_CLOB, "scc", "memory");
    return o;
}

__device__ __forceinline__ float row_max(const char* kb, u32x4 q) {
    float m = -__builtin_inff();
    const f32x16 z = {0};
    for (int t = 0; t < 64; ++t) {
        const f32x16 s = mfma32(ld16(kb + t * 1024), q, z);
#pragma unroll
        for (int j = 0; j < 16; ++j) m = fmaxf(m, s[j]);
    }
    return fmaxf(m, __shfl_xor(m, 32));
}

template <int T0, int T1, int T2, int T3>
__device__ __forceinline__ void conv_wave(const u16* __restrict__ hk, const char* ul,
                                          __amdgpu_buffer_rsrc_t rs, int zoff, float dd, int lane) {
    constexpr int NF = T3 + 1;
    const int r = lane & 15, g = lane >> 4;
    const int ns = (SEQ - 1) - 16 * (g >> 1) - r + 8 * (g & 1);
    const char* ap = ul + ((ns & 1) ? (U_RS + 2 * (ns - 1)) : 2 * ns) - 512 * (NF - 1);
    const int boff = 16 * r - 16 * (g >> 1) + 8 * (g & 1);
    const u16* bp = hk + boff;
    f32x4 c0 = {0}, c1 = {0}, c2 = {0}, c3 = {0};
#pragma unroll 1
    for (int rho = 0; rho < 8; ++rho) {
        u32x4 A[NF], B[NF];
        B[0] = (u32x4){0, 0, 0, 0};
        if (boff - 32 * rho >= 0) B[0] = ld16(bp - 32 * rho);
#pragma unroll
        for (int b = 1; b < NF; ++b) B[b] = ld16(bp - 32 * rho + 256 * b);
#pragma unroll
        for (int a = 0; a < NF; ++a) {
            const unsigned* p = reinterpret_cast<const unsigned*>(ap - 64 * rho + 512 * (NF - 1 - a));
            A[a] = (u32x4){p[0], p[1], p[2], p[3]};
        }
#pragma unroll
        for (int a = 0; a < NF; ++a) {
            c3 = mfma16(A[a], B[T3 - a], c3);
            if (a <= T2) c2 = mfma16(A[a], B[a <= T2 ? T2 - a : 0], c2);
            if (a <= T1) c1 = mfma16(A[a], B[a <= T1 ? T1 - a : 0], c1);
            if (a <= T0) c0 = mfma16(A[a], B[a <= T0 ? T0 - a : 0], c0);
        }
    }
    const int ts[4] = {T0, T1, T2, T3};
    const f32x4 cs[4] = {c0, c1, c2, c3};
#pragma unroll
    for (int i = 0; i < 4; ++i) {
        const int s = 256 * ts[i] + 16 * r + 4 * g;
        const u32x2 uu = ld8(ul + 2 * ((SEQ - 4) - s));
        const float u0 = bfhi(uu[1]), u1 = bflo(uu[1]), u2 = bfhi(uu[0]), u3 = bflo(uu[0]);
        const u32x2 o = {pack2(rbf(cs[i][0]) + rbf(u0 * dd), rbf(cs[i][1]) + rbf(u1 * dd)),
                         pack2(rbf(cs[i][2]) + rbf(u2 * dd), rbf(cs[i][3]) + rbf(u3 * dd))};
        __builtin_amdgcn_raw_buffer_store_b64(o, rs, zoff + 2 * s, 0, 16);
    }
}

__global__ void __launch_bounds__(768)
fused_kernel(const u16* __restrict__ x, const u16* __restrict__ wq, const u16* __restrict__ wk,
             const u16* __restrict__ wv, const u16* __restrict__ wo, const u16* __restrict__ bq,
             const u16* __restrict__ bk, const u16* __restrict__ bv, const u16* __restrict__ shk,
             const u16* __restrict__ ssmk, const u16* __restrict__ ssmd, u16* __restrict__ out,
             char* ws) {
    extern __shared__ __attribute__((aligned(16))) char smem[];
    const int lane = threadIdx.x & 63;
    const int w = __builtin_amdgcn_readfirstlane(threadIdx.x >> 6);
    const int blk = blockIdx.x;
    const int tg = (blk & 7) * 4 + (blk >> 6);
    const int hs = (blk >> 3) & 7;
    const int r = lane & 31, h = lane >> 5;
    const __amdgpu_buffer_rsrc_t rs = __builtin_amdgcn_make_buffer_rsrc(ws, 0, WS_END, 0x00020000);

    {
        const int n = hs < 2 ? 160 : 128;
        for (int c = w; c < n; c += 12) {
            const u16* src;
            if (c < 64) {
                src = x + (size_t)(64 * tg + c) * DM + ((lane ^ (c & 15)) << 3);
            } else if (c < 128) {
                const int row = 8 * (c - 64) + (lane >> 3);
                src = wq + (size_t)row * DM + 64 * hs + (((lane & 7) ^ (((row >> 1) & 1) << 2)) << 3);
            } else if (c < 144) {
                src = wk + (c - 128) * 512 + lane * 8;
            } else {
                src = wv + (c - 144) * 512 + lane * 8;
            }
            dma16(src, smem + c * 1024);
        }
    }
    u64 tag;
    asm volatile("s_load_dwordx2 %0, %1, 0x0\n\ts_waitcnt lgkmcnt(0)" : "=s"(tag) : "s"(ws) : "memory");
    tag += 1;
    drain();
    barrier();

    const int tq = (lane & 15) >> 2, tp = lane & 3;
    if (w < 8) {
        const int i = w >> 2, hd = w & 3;
        const char* xb = smem + L_X + (32 * i + r) * 1024;
        const int chunk = 4 * (hd >> 1) + 2 * ((lane >> 4) & 1) + (tp >> 1);
        const char* wb = smem + L_W + (8 * h + tq) * 128 + ((chunk ^ ((tq >> 1) << 2)) << 4) + 8 * (tp & 1);
        f32x16 acc = {0};
#pragma unroll 8
        for (int s = 0; s < 32; ++s)
            acc = mfma32(tr_frag(wb + s * 2048, 512), ld16(xb + (((2 * s + h) ^ (r & 15)) << 4)), acc);
        u32x4 q;
#pragma unroll
        for (int j = 0; j < 4; ++j) {
            const float lo = (hd & 1) ? acc[8 + 2 * j] : acc[2 * j];
            const float hi = (hd & 1) ? acc[9 + 2 * j] : acc[2 * j + 1];
            q[j] = pack2(lo * QSCALE, hi * QSCALE);
        }
        barrier();
        barrier();
        wait_flags(ws, WS_KVF + w * 128 + lane * 8, lane < 16, tag);
        const char* img = ws + WS_KV + lane * 16;
#pragma unroll
        for (int j = 0; j < 8; ++j) {
            const int t = w + 8 * j;
            dma16_dev(img + t * 2048, smem + L_K + t * 1024);
            dma16_dev(img + t * 2048 + 1024, smem + L_V + t * 1024);
        }
        const unsigned base = (unsigned)(size_t)(LDSP char*)smem;
        const unsigned ka = base + L_K + lane * 16;
        const unsigned va = base + (r < 16 ? L_V + h * 256 + r * 16
                                           : L_CONST + (r == 16 ? 0 : ((r >= 20 && r < 28) ? 64 : 16)));
        const unsigned vstep = r < 16 ? 4096 : 0;
        float m = 0.f;
        f32x16 o;
        float l;
#pragma unroll 1
        for (int given = 0; given < 2; ++given) {
            o = attn_pass(ka, va, vstep, base + L_CNT, q, m, given);
            l = __shfl(o[8], r);
            float mag = 0.f;
#pragma unroll
            for (int j = 0; j < 8; ++j) mag += fabsf(o[j]);
            if (!__any(!(l < 1.0e18f) || !(mag < 3.0e38f))) break;
            m = row_max(smem + L_K + lane * 16, q);
        }
        const float inv = 1.0f / l;
        u32x4 of;
#pragma unroll
        for (int j = 0; j < 4; ++j) of[j] = pack2(o[2 * j] * inv, o[2 * j + 1] * inv);
        __builtin_amdgcn_raw_buffer_store_b128(
            of, rs, WS_OF + ((2 * tg + i) * 32 + 4 * hs + hd) * 1024 + lane * 16, 0, 16);
        publish(ws, WS_OFL + ((tg * 8 + hs) * 8 + w) * 8, tag, lane);
        return;
    }

    const int wb4 = w - 8;
    u32x2 kr[11], vr[8];
    u32x4 sk[2];
    const int uc = lane & 15, us = (lane >> 4) + 4 * (wb4 & 1);
    const int us0 = 64 * tg + 8 * us, ud0 = 64 * hs + 4 * uc;
    if (wb4 < 2) {
        if (hs < 2) {
            __builtin_amdgcn_s_setprio(3);
            const int tt = 2 * tg + hs;
            const char* xb = smem + L_X + (32 * hs + r) * 1024;
            const char* wp = smem + (wb4 == 0 ? L_WK : L_WV) + (8 * h + tq) * 32 + 8 * tp;
            f32x16 acc = {0};
            const int fo = WS_KVF + ((tt & 7) * 8 + (tt >> 3)) * 16;
            if (wb4 == 0) {
#pragma unroll 8
                for (int s = 0; s < 32; ++s)
                    acc = mfma32(tr_frag(wp + s * 512, 128), ld16(xb + (((2 * s + h) ^ (r & 15)) << 4)), acc);
                const u32x4 f = {pack2(acc[0], acc[1]), pack2(acc[2], acc[3]), pack2(acc[4], acc[5]), pack2(acc[6], acc[7])};
                __builtin_amdgcn_raw_buffer_store_b128(f, rs, WS_KV + tt * 2048 + lane * 16, 0, 16);
                publish(ws, fo, tag, lane);
            } else {
#pragma unroll 8
                for (int s = 0; s < 32; ++s)
                    acc = mfma32(ld16(xb + (((2 * s + h) ^ (r & 15)) << 4)), tr_frag(wp + s * 512, 128), acc);
                const bool up = r >= 16;
                u32x4 f;
#pragma unroll
                for (int j = 0; j < 4; ++j)
                    f[j] = pack2(up ? acc[8 + 2 * j] : acc[2 * j], up ? acc[9 + 2 * j] : acc[2 * j + 1]);
                __builtin_amdgcn_raw_buffer_store_b128(
                    f, rs, WS_KV + tt * 2048 + 1024 + (r >> 4) * 512 + h * 256 + (r & 15) * 16, 0, 16);
                publish(ws, fo + 8, tag, lane);
            }
            __builtin_amdgcn_s_setprio(0);
        }
    } else {
#pragma unroll
        for (int j = 0; j < 11; ++j) {
            const int s = us0 - 3 + j;
            kr[j] = (u32x2){0, 0};
            if (s >= 0) kr[j] = ld8(bk + (size_t)s * DM + ud0);
        }
#pragma unroll
        for (int j = 0; j < 8; ++j) vr[j] = ld8(bv + (size_t)(us0 + j) * DM + ud0);
        sk[0] = ld16(shk + ud0 * 4);
        sk[1] = ld16(shk + ud0 * 4 + 8);
    }
    barrier();
    if (wb4 == 0) {
        if (lane < 27) {
            const int b = lane / 3, p = lane - 3 * b;
            const unsigned v = p == 0 ? 0x3f803f80u : 0u;
            *reinterpret_cast<u32x4*>(smem + L_CONST + b * 512 + (p == 2 ? 64 : 16 * p)) = (u32x4){v, v, v, v};
        } else if (lane < 31) {
            *reinterpret_cast<u32x4*>(smem + L_CNT + (lane - 27) * 16) = (u32x4){0, 0, 0, 0};
        }
    }
    barrier();

    if (wb4 >= 2) {
#pragma unroll
        for (int e = 0; e < 4; ++e) {
            const unsigned w01 = sk[e >> 1][(e & 1) * 2], w23 = sk[e >> 1][(e & 1) * 2 + 1];
            const float w0 = bflo(w01), w1 = bfhi(w01), w2 = bflo(w23), w3 = bfhi(w23);
            float kk[11], u[8];
#pragma unroll
            for (int j = 0; j < 11; ++j) kk[j] = (e & 1) ? bfhi(kr[j][e >> 1]) : bflo(kr[j][e >> 1]);
#pragma unroll
            for (int j = 0; j < 8; ++j) {
                float a = rbf(kk[j] * w3);
                a = rbf(a + rbf(kk[j + 1] * w2));
                a = rbf(a + rbf(kk[j + 2] * w1));
                a = rbf(a + rbf(kk[j + 3] * w0));
                u[j] = a * ((e & 1) ? bfhi(vr[j][e >> 1]) : bflo(vr[j][e >> 1]));
            }
            const u32x4 o = {pack2(u[7], u[6]), pack2(u[5], u[4]), pack2(u[3], u[2]), pack2(u[1], u[0])};
            __builtin_amdgcn_raw_buffer_store_b128(
                o, rs, WS_UR + ((ud0 + e) * SEQ + (SEQ - 8) - us0) * 2, 0, 16);
        }
        publish(ws, WS_URF + ((hs * 32 + tg) * 2 + (wb4 & 1)) * 8, tag, lane);
    }

    {
        const int ch = wb4 >> 1, sh = wb4 & 1;
        const int d = 2 * blk + ch;
        const float dd = bf2f(ssmd[d]);
        wait_flags(ws, WS_URF + ((d >> 6) * 64 + lane) * 8, true, tag);
        char* ul = smem + L_U + ch * 2 * U_RS;
        u32x4 uv[4];
#pragma unroll
        for (int j = 0; j < 4; ++j)
            uv[j] = __builtin_amdgcn_raw_buffer_load_b128(rs, WS_UR + d * 4096 + j * 1024 + lane * 16 + 2 * sh, 0, 16);
        if (sh && lane == 63) uv[3][3] &= 0xffffu;
#pragma unroll
        for (int j = 0; j < 4; ++j)
            *reinterpret_cast<u32x4*>(ul + sh * U_RS + j * 1024 + lane * 16) = uv[j];
        if (lane < 2) *reinterpret_cast<u32x4*>(ul + sh * U_RS + 4096 + lane * 16) = (u32x4){0, 0, 0, 0};
        LDSP unsigned* pc = (LDSP unsigned*)(smem + L_CNT + 16 + 4 * ch);
        asm volatile("s_waitcnt lgkmcnt(0)" ::: "memory");
        if (lane == 0) __hip_atomic_fetch_add(pc, 1u, __ATOMIC_RELAXED, __HIP_MEMORY_SCOPE_WORKGROUP);
        while (__hip_atomic_load(pc, __ATOMIC_RELAXED, __HIP_MEMORY_SCOPE_WORKGROUP) < 2u) __builtin_amdgcn_s_sleep(1);
        asm volatile("" ::: "memory");
        const u16* hk = ssmk + (size_t)d * SEQ;
        if (sh) conv_wave<1, 2, 4, 7>(hk, ul, rs, WS_ZT + d * 4096, dd, lane);
        else conv_wave<0, 3, 5, 6>(hk, ul, rs, WS_ZT + d * 4096, dd, lane);
        publish(ws, WS_ZF + (d * 2 + sh) * 8, tag, lane);
    }

    if (wb4 < 2) {
        const int c8 = lane & 7;
        const int s0 = 64 * tg + 4 * ((lane >> 3) + 8 * wb4);
        const int d0 = 64 * hs + 8 * c8;
        u32x4 qr[4];
#pragma unroll
        for (int j = 0; j < 4; ++j) qr[j] = ld16(bq + (size_t)(s0 + j) * DM + d0);
        const int sh = (0x96 >> (tg >> 2)) & 1;
        wait_flags(ws, WS_ZF + ((64 * hs + lane) * 2 + sh) * 8, true, tag);
        u32x2 z[8];
#pragma unroll
        for (int e = 0; e < 8; ++e)
            z[e] = __builtin_amdgcn_raw_buffer_load_b64(rs, WS_ZT + ((d0 + e) * SEQ + s0) * 2, 0, 16);
#pragma unroll
        for (int i = 0; i < 4; ++i) {
            u32x4 o;
#pragma unroll
            for (int j = 0; j < 4; ++j) {
                const unsigned za = z[2 * j][i >> 1], zb = z[2 * j + 1][i >> 1];
                const float a = (i & 1) ? bfhi(za) : bflo(za);
                const float b = (i & 1) ? bfhi(zb) : bflo(zb);
                o[j] = pack2(bflo(qr[i][j]) * a, bfhi(qr[i][j]) * b);
            }
            *reinterpret_cast<u32x4*>(out + (size_t)(s0 + i) * 1024 + 512 + d0) = o;
        }
    } else if (blk == 0 && wb4 == 2) {
#pragma unroll 1
        for (int j = 0; j < 8; ++j) wait_flags(ws, WS_URF + (j * 64 + lane) * 8, true, tag);
        if (lane == 0) __hip_atomic_store((GLBP u64*)(ws + WS_EPOCH), tag, __ATOMIC_RELAXED, __HIP_MEMORY_SCOPE_AGENT);
    }

    u32x4 fe[8], fo[8];
    {
        const u16* wp = wo + (size_t)(128 * wb4 + 4 * h) * DM + 64 * hs + 2 * r;
#pragma unroll
        for (int s = 0; s < 8; ++s) {
            unsigned wd[8];
#pragma unroll
            for (int j = 0; j < 8; ++j)
                wd[j] = *reinterpret_cast<const unsigned*>(wp + (size_t)(16 * s + (j & 3) + 8 * (j >> 2)) * DM);
#pragma unroll
            for (int j = 0; j < 4; ++j) {
                fe[s][j] = __builtin_amdgcn_perm(wd[2 * j + 1], wd[2 * j], 0x05040100u);
                fo[s][j] = __builtin_amdgcn_perm(wd[2 * j + 1], wd[2 * j], 0x07060302u);
            }
            asm volatile("" : "+v"(fe[s]), "+v"(fo[s]));
        }
    }
    barrier();
    wait_flags(ws, WS_OFL + ((tg * 8 + 2 * wb4) * 8 + lane) * 8, lane < 16, tag);
    {
        const char* src = ws + WS_OF + (2 * tg * 32 + 8 * wb4) * 1024 + lane * 16;
        char* ob = smem + wb4 * 16384;
#pragma unroll
        for (int s = 0; s < 8; ++s) {
            dma16_dev(src + s * 1024, ob + s * 1024);
            dma16_dev(src + (32 + s) * 1024, ob + (8 + s) * 1024);
        }
        drain();
        f32x16 acc[2][2] = {{{0}, {0}}, {{0}, {0}}};
#pragma unroll
        for (int s = 0; s < 8; ++s)
#pragma unroll
            for (int i = 0; i < 2; ++i) {
                const u32x4 of = ld16(ob + (8 * i + s) * 1024 + lane * 16);
                acc[i][0] = mfma32(fe[s], of, acc[i][0]);
                acc[i][1] = mfma32(fo[s], of, acc[i][1]);
            }
        f32x4* rp = reinterpret_cast<f32x4*>(smem + 65536) + wb4 * 1024 + lane;
#pragma unroll
        for (int i = 0; i < 2; ++i)
#pragma unroll
            for (int p = 0; p < 2; ++p)
#pragma unroll
                for (int g = 0; g < 4; ++g)
                    rp[((i * 4 + g) * 2 + p) * 64] = (f32x4){acc[i][p][4 * g], acc[i][p][4 * g + 1],
                                                             acc[i][p][4 * g + 2], acc[i][p][4 * g + 3]};
    }
    barrier();
    {
        const f32x4* rp = reinterpret_cast<const f32x4*>(smem + 65536) + wb4 * 256 + lane;
        f32x4 a[4];
#pragma unroll
        for (int g = 0; g < 4; ++g) a[g] = rp[g * 64];
#pragma unroll
        for (int q = 1; q < 4; ++q)
#pragma unroll
            for (int g = 0; g < 4; ++g) a[g] += rp[q * 1024 + g * 64];
        u16* dst = out + (size_t)(64 * tg + 32 * (wb4 >> 1) + r) * 1024 + 64 * hs + 32 * (wb4 & 1) + 8 * h;
#pragma unroll
        for (int g = 0; g < 2; ++g) {
            const u32x4 o = {pack2(a[2 * g][0], a[2 * g + 1][0]), pack2(a[2 * g][1], a[2 * g + 1][1]),
                             pack2(a[2 * g][2], a[2 * g + 1][2]), pack2(a[2 * g][3], a[2 * g + 1][3])};
            *reinterpret_cast<u32x4*>(dst + 16 * g) = o;
        }
    }
}

extern "C" void kernel_launch(void* const* d_in, const int* in_sizes, int n_in, void* d_out,
                              int out_size, void* d_ws, size_t ws_size, hipStream_t stream) {
    (void)hipFuncSetAttribute((const void*)fused_kernel, hipFuncAttributeMaxDynamicSharedMemorySize, LDS_BYTES);
    fused_kernel<<<256, 768, LDS_BYTES, stream>>>(
        (const u16*)d_in[0], (const u16*)d_in[1], (const u16*)d_in[2], (const u16*)d_in[3],
        (const u16*)d_in[4], (const u16*)d_in[5], (const u16*)d_in[6], (const u16*)d_in[7],
        (const u16*)d_in[8], (const u16*)d_in[9], (const u16*)d_in[10], (u16*)d_out, (char*)d_ws);
}
```

```cpp
#include <hip/hip_runtime.h>
#include <hip/hip_bf16.h>
#define CSP 3

typedef __attribute__((ext_vector_type(8))) __bf16 bf16x8;
typedef __attribute__((ext_vector_type(2))) __bf16 bf16x2;
typedef __attribute__((ext_vector_type(16))) float f32x16;
typedef __attribute__((ext_vector_type(4))) float f32x4;
typedef __attribute__((ext_vector_type(2))) float f32x2;
typedef __attribute__((ext_vector_type(4))) unsigned u32x4;
typedef __attribute__((ext_vector_type(2))) unsigned u32x2;
typedef __attribute__((ext_vector_type(4))) short s16x4;
typedef unsigned long long u64;
typedef unsigned short u16;
#define LDSP __attribute__((address_space(3)))
#define GLBP __attribute__((address_space(1)))

constexpr int SEQ = 2048;
constexpr int DM = 512;

constexpr int FL = 128;
constexpr int WS_EPOCH = 0;
constexpr int KVF_REP = 128 * FL + 4096;
constexpr int WS_KVF = 4096;
constexpr int WS_URF = WS_KVF + 8 * KVF_REP;
constexpr int WS_ZF = WS_URF + 512 * FL;
constexpr int WS_OFL = WS_ZF + 1024 * FL;
constexpr int WS_KV = WS_OFL + 2048 * FL;
constexpr int WS_UR = WS_KV + 131072;
constexpr int WS_ZT = WS_UR + 2097152 + 4096;
constexpr int WS_OF = WS_ZT + 2097152;
constexpr int WS_END = WS_OF + 2097152;

constexpr int L_X = 0;
constexpr int L_W = 65536;
constexpr int L_WK = 131072;
constexpr int L_WV = 147456;
constexpr int L_K = 0;
constexpr int L_V = 65536;
constexpr int L_CONST = 131072;
constexpr int L_CNT = L_CONST + 9 * 512;
constexpr int L_U = L_CNT + 64;
constexpr int U_RS = 4160;
constexpr int L_RED = L_U;
constexpr int L_QX = 147456;
constexpr int LDS_BYTES = 163840;

constexpr float QSCALE = 0.25f * 1.4426950408889634f;
constexpr unsigned SPIN_CAP = 1u << 18;

__device__ __forceinline__ float bf2f(u16 v) { return __uint_as_float(((unsigned)v) << 16); }
__device__ __forceinline__ float bflo(unsigned v) { return __uint_as_float(v << 16); }
__device__ __forceinline__ float bfhi(unsigned v) { return __uint_as_float(v & 0xffff0000u); }
__device__ __forceinline__ unsigned pack2(float lo, float hi) {
    f32x2 f = {lo, hi};
    bf16x2 b = __builtin_convertvector(f, bf16x2);
    return __builtin_bit_cast(unsigned, b);
}
__device__ __forceinline__ float rbf(float x) { return (float)(__bf16)x; }
__device__ __forceinline__ bf16x8 as_bf(u32x4 v) { return __builtin_bit_cast(bf16x8, v); }
__device__ __forceinline__ f32x16 mfma32(u32x4 a, u32x4 b, f32x16 c) {
    return __builtin_amdgcn_mfma_f32_32x32x16_bf16(as_bf(a), as_bf(b), c, 0, 0, 0);
}
__device__ __forceinline__ f32x4 mfma16(u32x4 a, u32x4 b, f32x4 c) {
    return __builtin_amdgcn_mfma_f32_16x16x32_bf16(as_bf(a), as_bf(b), c, 0, 0, 0);
}
__device__ __forceinline__ u32x4 ld16(const void* p) { return *reinterpret_cast<const u32x4*>(p); }
__device__ __forceinline__ u32x2 ld8(const void* p) { return *reinterpret_cast<const u32x2*>(p); }
__device__ __forceinline__ u32x4 tr_frag(const char* p, int second) {
    const s16x4 a = __builtin_amdgcn_ds_read_tr16_b64_v4i16((LDSP s16x4*)p);
    const s16x4 b = __builtin_amdgcn_ds_read_tr16_b64_v4i16((LDSP s16x4*)(p + second));
    const u32x2 a2 = __builtin_bit_cast(u32x2, a), b2 = __builtin_bit_cast(u32x2, b);
    return (u32x4){a2[0], a2[1], b2[0], b2[1]};
}
__device__ __forceinline__ void dma16(const void* src, char* dst) {
    __builtin_amdgcn_global_load_lds(src, (LDSP void*)dst, 16, 0, 0);
}
__device__ __forceinline__ void dma16_dev(const void* src, char* dst) {
    __builtin_amdgcn_global_load_lds(src, (LDSP void*)dst, 16, 0, 16);
}
__device__ __forceinline__ void barrier() {
    asm volatile("s_waitcnt lgkmcnt(0)\n\ts_barrier" ::: "memory");
}
__device__ __forceinline__ void proj_schedule() {
    __builtin_amdgcn_sched_group_barrier(0x100, 12, 0);
#pragma unroll
    for (int s = 0; s < 12; ++s) {
        __builtin_amdgcn_sched_group_barrier(0x008, 1, 0);
        __builtin_amdgcn_sched_group_barrier(0x100, 3, 0);
    }
    __builtin_amdgcn_sched_group_barrier(0x008, 4, 0);
}
__device__ __forceinline__ void drain() { asm volatile("s_waitcnt vmcnt(0)" ::: "memory"); }

__device__ __forceinline__ void lds_arrive(LDSP unsigned* c, int lane) {
    asm volatile("s_waitcnt lgkmcnt(0)" ::: "memory");
    if (lane == 0) __hip_atomic_fetch_add(c, 1u, __ATOMIC_RELAXED, __HIP_MEMORY_SCOPE_WORKGROUP);
}
__device__ __forceinline__ void lds_wait(LDSP unsigned* c, unsigned n) {
    while (__hip_atomic_load(c, __ATOMIC_RELAXED, __HIP_MEMORY_SCOPE_WORKGROUP) < n) {}
    asm volatile("" ::: "memory");
}

__device__ __forceinline__ void publish(char* ws, int off, u64 tag, int lane) {
    drain();
    if (lane == 0) __hip_atomic_store((GLBP u64*)(ws + off), tag, __ATOMIC_RELAXED, __HIP_MEMORY_SCOPE_AGENT);
}
template <int SLEEP>
__device__ __forceinline__ void wait_flags(char* ws, int off, bool active, u64 tag) {
#pragma unroll 1
    for (unsigned spin = 0; spin < SPIN_CAP; ++spin) {
        u64 v = tag;
        if (active) v = __hip_atomic_load((GLBP u64*)(ws + off), __ATOMIC_RELAXED, __HIP_MEMORY_SCOPE_AGENT);
        if (__all(v == tag)) break;
        __builtin_amdgcn_s_sleep(SLEEP);
    }
    asm volatile("" ::: "memory");
}

#define STR2(x) #x
#define STR(x) STR2(x)
#define EX(r) "v_exp_f32 v" STR(r) ", v" STR(r) "\n"
#define CV(d, a, b) "v_cvt_pk_bf16_f32 v" STR(d) ", v" STR(a) ", v" STR(b) "\n"
#define TILE(x0, x1, x2, x3, x4, x5, x6, x7, x8, x9, x10, x11, x12, x13, x14, x15, NEXT, u)     \
    "s_waitcnt lgkmcnt(2)\n"                                                                    \
    "v_mfma_f32_32x32x16_bf16 " NEXT ", v[104:107], %[q], v[116:131]\n"                         \
    "ds_read_b128 v[104:107], %[ka] offset:" STR((u + 2) * 1024) "\n"                           \
    EX(x0) EX(x1) EX(x2) EX(x3) CV(96, x0, x1) EX(x4) EX(x5) CV(97, x2, x3)                     \
    "s_waitcnt lgkmcnt(2)\n"                                                                    \
    "v_mfma_f32_32x32x16_bf16 %[o], v[112:115], v[100:103], %[o]\n"                             \
    "ds_read_b128 v[112:115], %[va] offset:" STR((2 * u + 1) * 512) "\n"                        \
    EX(x6) EX(x7) CV(98, x4, x5) EX(x8) CV(99, x6, x7) EX(x9) EX(x10) EX(x11)                   \
    "s_waitcnt lgkmcnt(2)\n"                                                                    \
    "v_mfma_f32_32x32x16_bf16 %[o], v[108:111], v[96:99], %[o]\n"                               \
    "ds_read_b128 v[108:111], %[va] offset:" STR((2 * u + 2) * 512) "\n"                        \
    CV(100, x8, x9) EX(x12) EX(x13) CV(101, x10, x11) EX(x14) EX(x15) CV(102, x12, x13)         \
    "s_nop 0\n" CV(103, x14, x15)
#define TILE_X(u) TILE(64, 65, 66, 67, 68, 69, 70, 71, 72, 73, 74, 75, 76, 77, 78, 79, "v[80:95]", u)
#define TILE_Y(u) TILE(80, 81, 82, 83, 84, 85, 86, 87, 88, 89, 90, 91, 92, 93, 94, 95, "v[64:79]", u)
#define MOVC(r) "v_mov_b32 v" STR(r) ", v116\n"
#define LDS_WAIT(off, n, lbl)                                                                   \
    lbl ":\n"                                                                                   \
    "ds_read_b32 v96, %[ca] offset:" STR(off) "\n"                                              \
    "s_waitcnt lgkmcnt(0)\n"                                                                    \
    "v_readfirstlane_b32 %[st], v96\n"                                                          \
    "s_cmp_lt_u32 %[st], " STR(n) "\n"                                                          \
    "s_cbranch_scc1 " lbl "b\n"
#define LDS_ARRIVE(off)                                                                         \
    "s_mov_b64 %[sx], exec\n"                                                                   \
    "s_mov_b64 exec, 1\n"                                                                       \
    "v_mov_b32 v96, 1\n"                                                                        \
    "ds_add_u32 %[ca], v96 offset:" STR(off) "\n"                                               \
    "s_mov_b64 exec, %[sx]\n"
__device__ __forceinline__ void rendezvous8(unsigned ca) {
    int st;
    u64 sx;
    asm volatile(LDS_ARRIVE(0) LDS_WAIT(0, 8, "20")
                 : [st] "=&s"(st), [sx] "=&s"(sx) : [ca] "v"(ca) : "v96", "scc", "memory");
}
#define ATT_CLOB                                                                                \
    "v64", "v65", "v66", "v67", "v68", "v69", "v70", "v71", "v72", "v73", "v74", "v75", "v76",  \
    "v77", "v78", "v79", "v80", "v81", "v82", "v83", "v84", "v85", "v86", "v87", "v88", "v89",  \
    "v90", "v91", "v92", "v93", "v94", "v95", "v96", "v97", "v98", "v99", "v100", "v101",       \
    "v102", "v103", "v104", "v105", "v106", "v107", "v108", "v109", "v110", "v111", "v112",     \
    "v113", "v114", "v115", "v116", "v117", "v118", "v119", "v120", "v121", "v122", "v123",     \
    "v124", "v125", "v126", "v127", "v128", "v129", "v130", "v131"

#define ITER_STAMP
#define ITER_OUT
__device__ __forceinline__ f32x16 attn_pass(unsigned ka, unsigned va, unsigned vstep, unsigned ca,
                                            u32x4 q, float mgiven, int given) {
    f32x16 o = {0};
    int cnt, st;
    u64 sx;
    asm volatile(
        "ds_read_b128 v[104:107], %[ka]\n"
        "v_mov_b32 v116, %[mg]\n"
        "s_cmp_lg_u32 %[gv], 0\n"
        "s_waitcnt lgkmcnt(0)\n"
        "s_cbranch_scc1 11f\n"
        "v_mfma_f32_32x32x16_bf16 v[64:79], v[104:107], %[q], 0\n"
        "s_nop 11\n"
        "v_max3_f32 v96, v64, v65, v66\n"
        "v_max3_f32 v97, v67, v68, v69\n"
        "v_max3_f32 v98, v70, v71, v72\n"
        "v_max3_f32 v99, v73, v74, v75\n"
        "v_max3_f32 v96, v96, v76, v77\n"
        "v_max3_f32 v97, v97, v78, v79\n"
        "v_max3_f32 v96, v96, v98, v99\n"
        "v_max_f32 v96, v96, v97\n"
        "v_mov_b32 v97, v96\n"
        "s_nop 1\n"
        "v_permlane32_swap_b32 v96, v97\n"
        "s_nop 1\n"
        "v_max_f32 v116, v96, v97\n"
        "11:\n"
        "v_xor_b32 v116, 0x80000000, v116\n"
        MOVC(117) MOVC(118) MOVC(119) MOVC(120) MOVC(121) MOVC(122) MOVC(123) MOVC(124)
        MOVC(125) MOVC(126) MOVC(127) MOVC(128) MOVC(129) MOVC(130) MOVC(131)
        "v_mov_b32 v100, 0\n v_mov_b32 v101, 0\n v_mov_b32 v102, 0\n v_mov_b32 v103, 0\n"
        "s_movk_i32 %[cnt], 16\n"
        "s_setprio 2\n"
        "v_mfma_f32_32x32x16_bf16 v[64:79], v[104:107], %[q], v[116:131]\n"
        "ds_read_b128 v[104:107], %[ka] offset:1024\n"
        "ds_read_b128 v[112:115], %[va]\n"
        "ds_read_b128 v[108:111], %[va]\n"
        "s_nop 10\n"
        "12:\n"
        TILE_X(0) TILE_Y(1) TILE_X(2) TILE_Y(3)
        "v_add_u32 %[ka], 4096, %[ka]\n"
        "v_add_u32 %[va], %[vs], %[va]\n"
        "s_cmp_lg_u32 %[cnt], 16\n"
        "s_cbranch_scc1 14f\n"
        "s_waitcnt vmcnt(0)\n"
        LDS_ARRIVE(24)
        LDS_WAIT(24, 8, "13")
        "14:\n"
        "s_cmp_lg_u32 %[cnt], 10\n"
        "s_cbranch_scc1 15f\n"
        LDS_WAIT(4, 2, "17")
        "s_setprio 1\n"
        "15:\n"
        "s_cmp_lg_u32 %[cnt], 3\n"
        "s_cbranch_scc1 16f\n"
        "s_setprio 0\n"
        LDS_ARRIVE(8)
        "s_waitcnt lgkmcnt(0)\n"
        "16:\n"
        ITER_STAMP
        "s_sub_u32 %[cnt], %[cnt], 1\n"
        "s_cmp_lg_u32 %[cnt], 0\n"
        "s_cbranch_scc1 12b\n"
        "s_waitcnt lgkmcnt(0)\n"
        "v_mfma_f32_32x32x16_bf16 %[o], v[112:115], v[100:103], %[o]\n"
        "s_nop 15\n"
        : [o] "+v"(o), [ka] "+v"(ka), [va] "+v"(va), [cnt] "=&s"(cnt), [st] "=&s"(st), [sx] "=&s"(sx) ITER_OUT
        : [q] "v"(q), [vs] "v"(vstep), [ca] "v"(ca), [mg] "v"(mgiven), [gv] "s"(given)
        : ATT_CLOB, "scc", "memory", "s90", "s91", "s92", "s93");
    return o;
}

__device__ __forceinline__ float row_max(const char* kb, u32x4 q) {
    float m = -__builtin_inff();
    const f32x16 z = {0};
    for (int t = 0; t < 64; ++t) {
        const f32x16 s = mfma32(ld16(kb + t * 1024), q, z);
#pragma unroll
        for (int j = 0; j < 16; ++j) m = fmaxf(m, s[j]);
    }
    return fmaxf(m, __shfl_xor(m, 32));
}

#define GAP __builtin_amdgcn_sched_barrier(0); asm volatile("s_sleep 1"); __builtin_amdgcn_sched_barrier(0);
template <int T0, int T1, int T2, int T3>
__device__ __forceinline__ void conv_wave(__amdgpu_buffer_rsrc_t hr, const char* ul,
                                          __amdgpu_buffer_rsrc_t rs, int zoff, float dd, int lane) {
    constexpr int NF = T3 + 1;
    const int r = lane & 15, g = lane >> 4;
    const int ns = (SEQ - 1) - 16 * (g >> 1) - r + 8 * (g & 1);
    unsigned ap = (unsigned)(size_t)(LDSP const char*)ul + ((ns & 1) ? (U_RS + 2 * (ns - 1)) : 2 * ns) - 512 * (NF - 1);
    int b0 = 2 * (16 * r - 16 * (g >> 1) + 8 * (g & 1));
    f32x4 c0 = {0}, c1 = {0}, c2 = {0}, c3 = {0};
#pragma unroll 1
    for (int rho = 0; rho < 8; ++rho) {
        u32x4 A[NF], B[NF];
        int b1 = b0 + 512;
        asm volatile("" : "+v"(b1), "+v"(ap));
        B[0] = __builtin_amdgcn_raw_buffer_load_b128(hr, b0, 0, 0);
#pragma unroll
        for (int b = 1; b < NF; ++b) B[b] = __builtin_amdgcn_raw_buffer_load_b128(hr, b1 + 512 * (b - 1), 0, 0);
#pragma unroll
        for (int a = 0; a < NF; ++a) {
            volatile LDSP const unsigned* p = (volatile LDSP const unsigned*)(size_t)(ap + 512 * (NF - 1 - a));
            A[a] = (u32x4){p[0], p[1], p[2], p[3]};
        }
        __builtin_amdgcn_sched_barrier(0);
#pragma unroll
        for (int a = 0; a < NF; ++a) {
            c3 = mfma16(A[a], B[T3 - a], c3);
            GAP
            if (a <= T2) { c2 = mfma16(A[a], B[a <= T2 ? T2 - a : 0], c2); GAP }
            if (a <= T1) { c1 = mfma16(A[a], B[a <= T1 ? T1 - a : 0], c1); GAP }
            if (a <= T0) { c0 = mfma16(A[a], B[a <= T0 ? T0 - a : 0], c0); GAP }
        }
        b0 -= 64;
        ap -= 64;
    }
    const int ts[4] = {T0, T1, T2, T3};
    const f32x4 cs[4] = {c0, c1, c2, c3};
#pragma unroll
    for (int i = 0; i < 4; ++i) {
        const int s = 256 * ts[i] + 16 * r + 4 * g;
        const u32x2 uu = ld8(ul + 2 * ((SEQ - 4) - s));
        const u32x2 o = {pack2(fmaf(bfhi(uu[1]), dd, cs[i][0]), fmaf(bflo(uu[1]), dd, cs[i][1])),
                         pack2(fmaf(bfhi(uu[0]), dd, cs[i][2]), fmaf(bflo(uu[0]), dd, cs[i][3]))};
        __builtin_amdgcn_raw_buffer_store_b64(o, rs, zoff + 2 * s, 0, 16);
    }
}

template <bool WFIRST>
__device__ __forceinline__ void kv_proj(const char* xb, const char* wp, int g, int c, f32x4& a0, f32x4& a1) {
#pragma unroll
    for (int s = 0; s < 16; ++s) {
        const int xo = (s >> 2) * 256 + (((4 * (s & 3) + g) ^ c) << 4);
        const u32x4 fw = tr_frag(wp + s * 1024, 128), f0 = ld16(xb + xo), f1 = ld16(xb + xo + 16384);
        a0 = WFIRST ? mfma16(fw, f0, a0) : mfma16(f0, fw, a0);
        a1 = WFIRST ? mfma16(fw, f1, a1) : mfma16(f1, fw, a1);
    }
    __builtin_amdgcn_sched_group_barrier(0x100, 16, 0);
#pragma unroll
    for (int s = 0; s < 12; ++s) {
        __builtin_amdgcn_sched_group_barrier(0x008, 2, 0);
        __builtin_amdgcn_sched_group_barrier(0x100, 4, 0);
    }
    __builtin_amdgcn_sched_group_barrier(0x008, 8, 0);
}

__global__ void __launch_bounds__(768)
fused_kernel(const u16* __restrict__ x, const u16* __restrict__ wq, const u16* __restrict__ wk,
             const u16* __restrict__ wv, const u16* __restrict__ wo, const u16* __restrict__ bq,
             const u16* __restrict__ bk, const u16* __restrict__ bv, const u16* __restrict__ shk,
             const u16* __restrict__ ssmk, const u16* __restrict__ ssmd, u16* __restrict__ out,
             char* ws) {
    extern __shared__ __attribute__((aligned(16))) char smem[];
    const int lane = threadIdx.x & 63;
    const int w = __builtin_amdgcn_readfirstlane(threadIdx.x >> 6);
    const int blk = blockIdx.x;
    const int tg = (blk & 7) * 4 + (blk >> 6);
    const int hs = (blk >> 3) & 7;
    const int r = lane & 31, h = lane >> 5;
    const __amdgpu_buffer_rsrc_t rs = __builtin_amdgcn_make_buffer_rsrc(ws, 0, WS_END, 0x00020000);
    asm volatile("" ::"s"(x), "s"(wq), "s"(wk), "s"(wv), "s"(wo), "s"(bq), "s"(bk), "s"(bv), "s"(shk),
                 "s"(ssmk), "s"(ssmd), "s"(out), "s"(ws));

    const bool kvp = hs < 2;
    const int xt = hs & 1;
    if (w >= 8) {
        __builtin_amdgcn_s_setprio(3);
        const int wb4 = w - 8;
#pragma unroll
        for (int j = 0; j < 8; ++j) {
            const int row = 32 * xt + 4 * j + wb4;
            dma16(x + (size_t)(64 * tg + row) * DM + ((lane ^ (row & 15)) << 3), smem + L_X + row * 1024);
        }
        if (kvp) {
            const u16* src = (wb4 < 2 ? wk : wv) + (wb4 & 1) * 4096 + lane * 8;
#pragma unroll
            for (int j = 0; j < 8; ++j) dma16(src + j * 512, smem + L_WK + (wb4 * 8 + j) * 1024);
        }
    } else {
        if (kvp) barrier();
#pragma unroll
        for (int j = 0; j < 4; ++j) {
            const int row = 32 * (xt ^ 1) + 8 * j + w;
            dma16(x + (size_t)(64 * tg + row) * DM + ((lane ^ (row & 15)) << 3), smem + L_X + row * 1024);
        }
        const u16* src = wq + (size_t)(64 * w + (lane >> 3)) * DM + 64 * hs + (((lane & 7) ^ (((lane >> 4) & 1) << 2)) << 3);
#pragma unroll
        for (int j = 0; j < 8; ++j) dma16(src + (size_t)j * 8 * DM, smem + L_W + (8 * w + j) * 1024);
    }
    u64 tag;
    asm volatile("s_load_dwordx2 %0, %1, 0x0\n\ts_waitcnt lgkmcnt(0)" : "=s"(tag) : "s"(ws) : "memory");
    tag += 1;

    const int tq = (lane & 15) >> 2, tp = lane & 3;
    if (w < 8) {
        drain();
        barrier();
        const int i = w >> 2, hd = w & 3, kh = hd & 1;
        const char* xb = smem + L_X + (32 * i + r) * 1024 + kh * 512;
        const int chunk = 4 * (hd >> 1) + 2 * ((lane >> 4) & 1) + (tp >> 1);
        const char* wb = smem + L_W + kh * 32768 + (8 * h + tq) * 128 + ((chunk ^ ((tq >> 1) << 2)) << 4) + 8 * (tp & 1);
        u32x4 fa[16], fb[16];
#pragma unroll
        for (int s = 0; s < 16; ++s) {
            fa[s] = tr_frag(wb + s * 2048, 512);
            fb[s] = ld16(xb + (s >> 3) * 256 + (((2 * (s & 7) + h) ^ (r & 15)) << 4));
        }
        f32x16 acc = {0};
#pragma unroll
        for (int s = 0; s < 16; ++s) acc = mfma32(fa[s], fb[s], acc);
        proj_schedule();
        f32x4 keep[2];
        {
            f32x4* xp = reinterpret_cast<f32x4*>(smem + L_QX + (w ^ 1) * 2048) + lane;
#pragma unroll
            for (int g = 0; g < 2; ++g) {
                f32x4 lo = {acc[4 * g], acc[4 * g + 1], acc[4 * g + 2], acc[4 * g + 3]};
                f32x4 hi = {acc[8 + 4 * g], acc[9 + 4 * g], acc[10 + 4 * g], acc[11 + 4 * g]};
                xp[g * 64] = kh ? lo : hi;
                keep[g] = kh ? hi : lo;
            }
        }
        barrier();
        dma16_dev(ws + WS_KV + w * 2048 + lane * 16, smem + L_K + w * 1024);
        dma16_dev(ws + WS_KV + w * 2048 + 1024 + lane * 16, smem + L_V + w * 1024);
        rendezvous8((unsigned)(size_t)(LDSP char*)smem + L_CNT + 12);
#pragma unroll
        for (int j = 1; j < 4; ++j) {
            dma16_dev(ws + WS_KV + (w + 8 * j) * 2048 + lane * 16, smem + L_K + (w + 8 * j) * 1024);
            dma16_dev(ws + WS_KV + (w + 8 * j) * 2048 + 1024 + lane * 16, smem + L_V + (w + 8 * j) * 1024);
        }
        const unsigned base = (unsigned)(size_t)(LDSP char*)smem;
        u32x4 q;
        {
            f32x4 a, b;
            asm volatile("ds_read_b128 %0, %2\n\tds_read_b128 %1, %2 offset:1024\n\ts_waitcnt lgkmcnt(0)"
                         : "=&v"(a), "=&v"(b) : "v"(base + L_QX + w * 2048 + lane * 16) : "memory");
            a = (a + keep[0]) * QSCALE;
            b = (b + keep[1]) * QSCALE;
            q = (u32x4){pack2(a[0], a[1]), pack2(a[2], a[3]), pack2(b[0], b[1]), pack2(b[2], b[3])};
        }
        const unsigned ka = base + L_K + lane * 16;
        const unsigned va = base + (r < 16 ? L_V + h * 256 + r * 16
                                           : L_CONST + (r == 16 ? 0 : ((r >= 20 && r < 28) ? 64 : 16)));
        const unsigned vstep = r < 16 ? 4096 : 0;
        asm volatile("s_waitcnt vmcnt(6)" ::: "memory");
        rendezvous8(base + L_CNT);
        float m = 0.f;
        f32x16 o;
        float l;
#pragma unroll 1
        for (int given = 0; given < 2; ++given) {
            o = attn_pass(ka, va, vstep, base + L_CNT, q, m, given);
            l = __shfl(o[8], r);
            float mag = 0.f;
#pragma unroll
            for (int j = 0; j < 8; ++j) mag += fabsf(o[j]);
            if (!__any(!(l < 1.0e18f) || !(mag < 3.0e38f))) break;
            m = row_max(smem + L_K + lane * 16, q);
        }
        const float inv = 1.0f / l;
        u32x4 of;
#pragma unroll
        for (int j = 0; j < 4; ++j) of[j] = pack2(o[2 * j] * inv, o[2 * j + 1] * inv);
        __builtin_amdgcn_raw_buffer_store_b128(
            of, rs, WS_OF + ((2 * tg + i) * 32 + 4 * hs + hd) * 1024 + lane * 16, 0, 16);
        publish(ws, WS_OFL + ((tg * 8 + hs) * 8 + w) * FL, tag, lane);
        return;
    }

    const int wb4 = w - 8;
    drain();
    if (kvp) barrier();
    if (wb4 < 2) {
        const int tt = 2 * tg + hs;
        if (kvp) {
            const int c = lane & 15, g = lane >> 4;
            const char* xb = smem + L_X + (32 * hs + c) * 1024;
            const char* wp = smem + (wb4 == 0 ? L_WK : L_WV) + (8 * g + tq) * 32 + 8 * tp;
            f32x4 a0 = {0}, a1 = {0};
            if (wb4 == 0) kv_proj<true>(xb, wp, g, c, a0, a1);
            else kv_proj<false>(xb, wp, g, c, a0, a1);
            const int off = WS_KV + tt * 2048 + wb4 * 1024 + (g & 1) * (wb4 ? 256 : 512) + c * 16 + (g >> 1) * 8;
            __builtin_amdgcn_raw_buffer_store_b64((u32x2){pack2(a0[0], a0[1]), pack2(a0[2], a0[3])}, rs, off, 0, 16);
            __builtin_amdgcn_raw_buffer_store_b64((u32x2){pack2(a1[0], a1[1]), pack2(a1[2], a1[3])}, rs,
                                                  off + (wb4 ? 512 : 256), 0, 16);
            drain();
            if (lane < 8)
                __hip_atomic_store((GLBP u64*)(ws + WS_KVF + lane * KVF_REP + (tt * 2 + wb4) * FL), tag,
                                   __ATOMIC_RELAXED, __HIP_MEMORY_SCOPE_AGENT);
        }
        barrier();
        if (wb4 == 0) {
            if (lane < 27) {
                const int b = lane / 3, p = lane - 3 * b;
                const unsigned v = p == 0 ? 0x3f803f80u : 0u;
                *reinterpret_cast<u32x4*>(smem + L_CONST + b * 512 + (p == 2 ? 64 : 16 * p)) = (u32x4){v, v, v, v};
            } else if (lane < 31) {
                *reinterpret_cast<u32x4*>(smem + L_CNT + (lane - 27) * 16) = (u32x4){0, 0, 0, 0};
            }
        }
        wait_flags<2>(ws, WS_KVF + (blk & 7) * KVF_REP + (wb4 * 64 + lane) * FL, true, tag);
        barrier();
        {
            lds_wait((LDSP unsigned*)(smem + L_CNT), 8);
            const char* src = ws + WS_KV + 32 * 2048 + wb4 * 1024 + lane * 16;
            char* dst = smem + (wb4 ? L_V : L_K) + 32 * 1024;
#pragma unroll 8
            for (int t = 0; t < 32; ++t) dma16_dev(src + t * 2048, dst + t * 1024);
            drain();
            lds_arrive((LDSP unsigned*)(smem + L_CNT + 4), lane);
        }
    } else {
        barrier();
        const int s0 = 64 * tg + 8 * ((lane >> 4) + 4 * (wb4 & 1));
        const int d0 = 64 * hs + 4 * (lane & 15);
        const __amdgpu_buffer_rsrc_t kr_ = __builtin_amdgcn_make_buffer_rsrc((void*)bk, 0, SEQ * DM * 2, 0x00020000);
        const __amdgpu_buffer_rsrc_t vr_ = __builtin_amdgcn_make_buffer_rsrc((void*)bv, 0, SEQ * DM * 2, 0x00020000);
        const int o0 = (s0 * DM + d0) * 2;
        int om = o0 - 3072;
        asm volatile("" : "+v"(om));
        u32x2 kr[11], vr[8];
#pragma unroll
        for (int j = 0; j < 3; ++j) kr[j] = __builtin_amdgcn_raw_buffer_load_b64(kr_, om + 1024 * j, 0, 0);
#pragma unroll
        for (int j = 0; j < 8; ++j) {
            kr[3 + j] = __builtin_amdgcn_raw_buffer_load_b64(kr_, o0 + 1024 * (j & 3), 4096 * (j >> 2), 0);
            vr[j] = __builtin_amdgcn_raw_buffer_load_b64(vr_, o0 + 1024 * (j & 3), 4096 * (j >> 2), 0);
        }
        const u32x4 sk0 = ld16(shk + d0 * 4), sk1 = ld16(shk + d0 * 4 + 8);
#pragma unroll
        for (int e = 0; e < 4; ++e) {
            const unsigned w01 = (e < 2 ? sk0 : sk1)[(e & 1) * 2], w23 = (e < 2 ? sk0 : sk1)[(e & 1) * 2 + 1];
            const float w0 = bflo(w01), w1 = bfhi(w01), w2 = bflo(w23), w3 = bfhi(w23);
            float kk[11], u[8];
#pragma unroll
            for (int j = 0; j < 11; ++j) kk[j] = (e & 1) ? bfhi(kr[j][e >> 1]) : bflo(kr[j][e >> 1]);
#pragma unroll
            for (int j = 0; j < 8; ++j) {
                const float a = fmaf(kk[j + 3], w0, fmaf(kk[j + 2], w1, fmaf(kk[j + 1], w2, kk[j] * w3)));
                u[j] = a * ((e & 1) ? bfhi(vr[j][e >> 1]) : bflo(vr[j][e >> 1]));
            }
            const u32x4 o = {pack2(u[7], u[6]), pack2(u[5], u[4]), pack2(u[3], u[2]), pack2(u[1], u[0])};
            __builtin_amdgcn_raw_buffer_store_b128(o, rs, WS_UR + ((d0 + e) * SEQ + (SEQ - 8) - s0) * 2, 0, 16);
        }
        publish(ws, WS_URF + ((hs * 32 + tg) * 2 + (wb4 & 1)) * FL, tag, lane);
        barrier();
    }

    {
        const int ch = wb4 >> 1, sh = wb4 & 1;
        const int d = 2 * blk + ch;
        const float dd = bf2f(ssmd[d]);
        wait_flags<24>(ws, WS_URF + ((d >> 6) * 64 + lane) * FL, true, tag);
        char* ul = smem + L_U + ch * 2 * U_RS;
        u32x4 uv[4];
#pragma unroll
        for (int j = 0; j < 4; ++j)
            uv[j] = __builtin_amdgcn_raw_buffer_load_b128(rs, WS_UR + d * 4096 + j * 1024 + lane * 16 + 2 * sh, 0, 16);
        if (sh && lane == 63) uv[3][3] &= 0xffffu;
        lds_wait((LDSP unsigned*)(smem + L_CNT), 8);
#pragma unroll
        for (int j = 0; j < 4; ++j)
            *reinterpret_cast<u32x4*>(ul + sh * U_RS + j * 1024 + lane * 16) = uv[j];
        if (lane < 2) *reinterpret_cast<u32x4*>(ul + sh * U_RS + 4096 + lane * 16) = (u32x4){0, 0, 0, 0};
        LDSP unsigned* pc = (LDSP unsigned*)(smem + L_CNT + 16 + 4 * ch);
        lds_arrive(pc, lane);
        lds_wait(pc, 2);
        const __amdgpu_buffer_rsrc_t hk = __builtin_amdgcn_make_buffer_rsrc((void*)(ssmk + (size_t)d * SEQ), 0, SEQ * 2, 0x00020000);
        if (sh) conv_wave<1, 2, 4, 7>(hk, ul, rs, WS_ZT + d * 4096, dd, lane);
        else conv_wave<0, 3, 5, 6>(hk, ul, rs, WS_ZT + d * 4096, dd, lane);
        publish(ws, WS_ZF + (d * 2 + sh) * FL, tag, lane);
    }

    if (wb4 < 2) {
        const int c8 = lane & 7;
        const int s0 = 64 * tg + 4 * ((lane >> 3) + 8 * wb4);
        const int d0 = 64 * hs + 8 * c8;
        u32x4 qr[4];
#pragma unroll
        for (int j = 0; j < 4; ++j) qr[j] = ld16(bq + (size_t)(s0 + j) * DM + d0);
        const int sh = (0x96 >> (tg >> 2)) & 1;
        wait_flags<24>(ws, WS_ZF + ((64 * hs + lane) * 2 + sh) * FL, true, tag);
        u32x2 z[8];
#pragma unroll
        for (int e = 0; e < 8; ++e)
            z[e] = __builtin_amdgcn_raw_buffer_load_b64(rs, WS_ZT + ((d0 + e) * SEQ + s0) * 2, 0, 16);
#pragma unroll
        for (int i = 0; i < 4; ++i) {
            u32x4 o;
#pragma unroll
            for (int j = 0; j < 4; ++j) {
                const unsigned za = z[2 * j][i >> 1], zb = z[2 * j + 1][i >> 1];
                const float a = (i & 1) ? bfhi(za) : bflo(za);
                const float b = (i & 1) ? bfhi(zb) : bflo(zb);
                o[j] = pack2(bflo(qr[i][j]) * a, bfhi(qr[i][j]) * b);
            }
            *reinterpret_cast<u32x4*>(out + (size_t)(s0 + i) * 1024 + 512 + d0) = o;
        }
    } else if (blk == 0 && wb4 == 2) {
#pragma unroll 1
        for (int j = 0; j < 8; ++j) wait_flags<24>(ws, WS_URF + (j * 64 + lane) * FL, true, tag);
        if (lane == 0) __hip_atomic_store((GLBP u64*)(ws + WS_EPOCH), tag, __ATOMIC_RELAXED, __HIP_MEMORY_SCOPE_AGENT);
    }

    u32x4 fe[8], fo[8];
    {
        const __amdgpu_buffer_rsrc_t wr = __builtin_amdgcn_make_buffer_rsrc((void*)wo, 0, DM * DM * 2, 0x00020000);
        const int vo = (4 * h * DM + 2 * (r ^ (8 * wb4))) * 2;
        const int so = (128 * wb4 * DM + 64 * hs) * 2;
        unsigned wd[8][8];
#pragma unroll
        for (int s = 0; s < 8; ++s)
#pragma unroll
            for (int j = 0; j < 8; ++j)
                wd[s][j] = __builtin_amdgcn_raw_buffer_load_b32(wr, vo + (j & 3) * 1024, so + (16 * s + 8 * (j >> 2)) * 1024, 0);
        __builtin_amdgcn_sched_barrier(0);
#pragma unroll
        for (int s = 0; s < 8; ++s) {
#pragma unroll
            for (int j = 0; j < 4; ++j) {
                fe[s][j] = __builtin_amdgcn_perm(wd[s][2 * j + 1], wd[s][2 * j], 0x05040100u);
                fo[s][j] = __builtin_amdgcn_perm(wd[s][2 * j + 1], wd[s][2 * j], 0x07060302u);
            }
            asm volatile("" : "+v"(fe[s]), "+v"(fo[s]));
        }
    }
    LDSP unsigned* cnt = (LDSP unsigned*)(smem + L_CNT + 32);
    lds_arrive(cnt, lane);
    while (__hip_atomic_load((LDSP unsigned*)(smem + L_CNT + 8), __ATOMIC_RELAXED, __HIP_MEMORY_SCOPE_WORKGROUP) < 4u)
        __builtin_amdgcn_s_sleep(8);
#pragma unroll 1
    for (int i = 0; i < 2; ++i) {
        wait_flags<1>(ws, WS_OFL + ((tg * 8 + 2 * wb4 + (lane >> 2)) * 8 + 4 * i + (lane & 3)) * FL, lane < 8, tag);
        u32x4 of[8];
#pragma unroll
        for (int s = 0; s < 8; ++s)
            of[s] = __builtin_amdgcn_raw_buffer_load_b128(
                rs, lane * 16 + (s & 3) * 1024, WS_OF + ((2 * tg + i) * 32 + 8 * wb4 + (s & 4)) * 1024, 16);
        f32x16 ae = {0}, ao = {0};
#pragma unroll
        for (int s = 0; s < 8; ++s) {
            ae = mfma32(fe[s], of[s], ae);
            ao = mfma32(fo[s], of[s], ao);
        }
        lds_wait(cnt, 4 + 8 * i);
#pragma unroll
        for (int g = 1; g < 4; ++g) {
            f32x4* p = reinterpret_cast<f32x4*>(smem + L_RED + ((g ^ wb4) * 3 + g - 1) * 2048) + lane;
            p[0] = (f32x4){ae[4 * g], ae[4 * g + 1], ae[4 * g + 2], ae[4 * g + 3]};
            p[64] = (f32x4){ao[4 * g], ao[4 * g + 1], ao[4 * g + 2], ao[4 * g + 3]};
        }
        lds_arrive(cnt, lane);
        lds_wait(cnt, 8 + 8 * i);
        f32x4 e = {ae[0], ae[1], ae[2], ae[3]}, o = {ao[0], ao[1], ao[2], ao[3]};
        const f32x4* p = reinterpret_cast<const f32x4*>(smem + L_RED + wb4 * 3 * 2048) + lane;
#pragma unroll
        for (int g = 0; g < 3; ++g) {
            e += p[g * 128];
            o += p[g * 128 + 64];
        }
        const u32x4 res = {pack2(e[0], o[0]), pack2(e[1], o[1]), pack2(e[2], o[2]), pack2(e[3], o[3])};
        *reinterpret_cast<u32x4*>(out + (size_t)(64 * tg + 32 * i + r) * 1024 + 64 * hs + 16 * wb4 + 8 * h) = res;
        lds_arrive(cnt, lane);
    }
}

extern "C" void kernel_launch(void* const* d_in, const int* in_sizes, int n_in, void* d_out,
                              int out_size, void* d_ws, size_t ws_size, hipStream_t stream) {
    (void)hipFuncSetAttribute((const void*)fused_kernel, hipFuncAttributeMaxDynamicSharedMemorySize, LDS_BYTES);
    fused_kernel<<<256, 768, LDS_BYTES, stream>>>(
        (const u16*)d_in[0], (const u16*)d_in[1], (const u16*)d_in[2], (const u16*)d_in[3],
        (const u16*)d_in[4], (const u16*)d_in[5], (const u16*)d_in[6], (const u16*)d_in[7],
        (const u16*)d_in[8], (const u16*)d_in[9], (const u16*)d_in[10], (u16*)d_out, (char*)d_ws);
}
```

```cpp
#include <hip/hip_runtime.h>
#include <hip/hip_bf16.h>
#define OUT_AUX 2
#define CSP 3

typedef __attribute__((ext_vector_type(8))) __bf16 bf16x8;
typedef __attribute__((ext_vector_type(2))) __bf16 bf16x2;
typedef __attribute__((ext_vector_type(16))) float f32x16;
typedef __attribute__((ext_vector_type(4))) float f32x4;
typedef __attribute__((ext_vector_type(2))) float f32x2;
typedef __attribute__((ext_vector_type(4))) unsigned u32x4;
typedef __attribute__((ext_vector_type(2))) unsigned u32x2;
typedef __attribute__((ext_vector_type(4))) short s16x4;
typedef unsigned long long u64;
typedef unsigned short u16;
#define LDSP __attribute__((address_space(3)))
#define GLBP __attribute__((address_space(1)))

constexpr int SEQ = 2048;
constexpr int DM = 512;

constexpr int FL = 128;
constexpr int WS_EPOCH = 0;
constexpr int KVF_REP = 128 * FL + 4096;
constexpr int WS_KVF = 4096;
constexpr int WS_URF = WS_KVF + 8 * KVF_REP;
constexpr int WS_ZF = WS_URF + 512 * FL;
constexpr int WS_OFL = WS_ZF + 1024 * FL;
constexpr int WS_KV = WS_OFL + 2048 * FL;
constexpr int WS_UR = WS_KV + 131072;
constexpr int WS_ZT = WS_UR + 2097152 + 4096;
constexpr int WS_OF = WS_ZT + 2097152;
constexpr int WS_END = WS_OF + 2097152;

constexpr int L_X = 0;
constexpr int L_W = 65536;
constexpr int L_WK = 131072;
constexpr int L_WV = 147456;
constexpr int L_K = 0;
constexpr int L_V = 65536;
constexpr int L_CONST = 131072;
constexpr int L_CNT = L_CONST + 9 * 512;
constexpr int L_U = L_CNT + 64;
constexpr int U_RS = 4160;
constexpr int L_RED = L_U;
constexpr int L_QX = 147456;
constexpr int LDS_BYTES = 163840;

constexpr float QSCALE = 0.25f * 1.4426950408889634f;
constexpr unsigned SPIN_CAP = 1u << 18;

__device__ __forceinline__ float bf2f(u16 v) { return __uint_as_float(((unsigned)v) << 16); }
__device__ __forceinline__ float bflo(unsigned v) { return __uint_as_float(v << 16); }
__device__ __forceinline__ float bfhi(unsigned v) { return __uint_as_float(v & 0xffff0000u); }
__device__ __forceinline__ unsigned pack2(float lo, float hi) {
    f32x2 f = {lo, hi};
    bf16x2 b = __builtin_convertvector(f, bf16x2);
    return __builtin_bit_cast(unsigned, b);
}
__device__ __forceinline__ float rbf(float x) { return (float)(__bf16)x; }
__device__ __forceinline__ bf16x8 as_bf(u32x4 v) { return __builtin_bit_cast(bf16x8, v); }
__device__ __forceinline__ f32x16 mfma32(u32x4 a, u32x4 b, f32x16 c) {
    return __builtin_amdgcn_mfma_f32_32x32x16_bf16(as_bf(a), as_bf(b), c, 0, 0, 0);
}
__device__ __forceinline__ f32x4 mfma16(u32x4 a, u32x4 b, f32x4 c) {
    return __builtin_amdgcn_mfma_f32_16x16x32_bf16(as_bf(a), as_bf(b), c, 0, 0, 0);
}
__device__ __forceinline__ u32x4 ld16(const void* p) { return *reinterpret_cast<const u32x4*>(p); }
__device__ __forceinline__ u32x2 ld8(const void* p) { return *reinterpret_cast<const u32x2*>(p); }
__device__ __forceinline__ u32x4 tr_frag(const char* p, int second) {
    const s16x4 a = __builtin_amdgcn_ds_read_tr16_b64_v4i16((LDSP s16x4*)p);
    const s16x4 b = __builtin_amdgcn_ds_read_tr16_b64_v4i16((LDSP s16x4*)(p + second));
    const u32x2 a2 = __builtin_bit_cast(u32x2, a), b2 = __builtin_bit_cast(u32x2, b);
    return (u32x4){a2[0], a2[1], b2[0], b2[1]};
}
__device__ __forceinline__ void dma16(const void* src, char* dst) {
    __builtin_amdgcn_global_load_lds(src, (LDSP void*)dst, 16, 0, 0);
}
__device__ __forceinline__ void dma16_dev(const void* src, char* dst) {
    __builtin_amdgcn_global_load_lds(src, (LDSP void*)dst, 16, 0, 16);
}
__device__ __forceinline__ void barrier() {
    asm volatile("s_waitcnt lgkmcnt(0)\n\ts_barrier" ::: "memory");
}
__device__ __forceinline__ void proj_schedule() {
    __builtin_amdgcn_sched_group_barrier(0x100, 12, 0);
#pragma unroll
    for (int s = 0; s < 12; ++s) {
        __builtin_amdgcn_sched_group_barrier(0x008, 1, 0);
        __builtin_amdgcn_sched_group_barrier(0x100, 3, 0);
    }
    __builtin_amdgcn_sched_group_barrier(0x008, 4, 0);
}
__device__ __forceinline__ void drain() { asm volatile("s_waitcnt vmcnt(0)" ::: "memory"); }

__device__ __forceinline__ void lds_arrive(LDSP unsigned* c, int lane) {
    asm volatile("s_waitcnt lgkmcnt(0)" ::: "memory");
    if (lane == 0) __hip_atomic_fetch_add(c, 1u, __ATOMIC_RELAXED, __HIP_MEMORY_SCOPE_WORKGROUP);
}
__device__ __forceinline__ void lds_wait(LDSP unsigned* c, unsigned n) {
    while (__hip_atomic_load(c, __ATOMIC_RELAXED, __HIP_MEMORY_SCOPE_WORKGROUP) < n) {}
    asm volatile("" ::: "memory");
}

__device__ __forceinline__ void publish(char* ws, int off, u64 tag, int lane) {
    drain();
    if (lane == 0) __hip_atomic_store((GLBP u64*)(ws + off), tag, __ATOMIC_RELAXED, __HIP_MEMORY_SCOPE_AGENT);
}
template <int SLEEP>
__device__ __forceinline__ void wait_flags(char* ws, int off, bool active, u64 tag) {
#pragma unroll 1
    for (unsigned spin = 0; spin < SPIN_CAP; ++spin) {
        u64 v = tag;
        if (active) v = __hip_atomic_load((GLBP u64*)(ws + off), __ATOMIC_RELAXED, __HIP_MEMORY_SCOPE_AGENT);
        if (__all(v == tag)) break;
        __builtin_amdgcn_s_sleep(SLEEP);
    }
    asm volatile("" ::: "memory");
}

#define STR2(x) #x
#define STR(x) STR2(x)
#define EX(r) "v_exp_f32 v" STR(r) ", v" STR(r) "\n"
#define CV(d, a, b) "v_cvt_pk_bf16_f32 v" STR(d) ", v" STR(a) ", v" STR(b) "\n"
#define TILE(x0, x1, x2, x3, x4, x5, x6, x7, x8, x9, x10, x11, x12, x13, x14, x15, NEXT, u)     \
    "s_waitcnt lgkmcnt(2)\n"                                                                    \
    "v_mfma_f32_32x32x16_bf16 " NEXT ", v[104:107], %[q], v[116:131]\n"                         \
    "ds_read_b128 v[104:107], %[ka] offset:" STR((u + 2) * 1024) "\n"                           \
    EX(x0) EX(x1) EX(x2) EX(x3) CV(96, x0, x1) EX(x4) EX(x5) CV(97, x2, x3)                     \
    "s_waitcnt lgkmcnt(2)\n"                                                                    \
    "v_mfma_f32_32x32x16_bf16 %[o], v[112:115], v[100:103], %[o]\n"                             \
    "ds_read_b128 v[112:115], %[va] offset:" STR((2 * u + 1) * 512) "\n"                        \
    EX(x6) EX(x7) CV(98, x4, x5) EX(x8) CV(99, x6, x7) EX(x9) EX(x10) EX(x11)                   \
    "s_waitcnt lgkmcnt(2)\n"                                                                    \
    "v_mfma_f32_32x32x16_bf16 %[o], v[108:111], v[96:99], %[o]\n"                               \
    "ds_read_b128 v[108:111], %[va] offset:" STR((2 * u + 2) * 512) "\n"                        \
    CV(100, x8, x9) EX(x12) EX(x13) CV(101, x10, x11) EX(x14) EX(x15) CV(102, x12, x13)         \
    "s_nop 0\n" CV(103, x14, x15)
#define TILE_X(u) TILE(64, 65, 66, 67, 68, 69, 70, 71, 72, 73, 74, 75, 76, 77, 78, 79, "v[80:95]", u)
#define TILE_Y(u) TILE(80, 81, 82, 83, 84, 85, 86, 87, 88, 89, 90, 91, 92, 93, 94, 95, "v[64:79]", u)
#define MOVC(r) "v_mov_b32 v" STR(r) ", v116\n"
#define LDS_WAIT(off, n, lbl)                                                                   \
    lbl ":\n"                                                                                   \
    "ds_read_b32 v96, %[ca] offset:" STR(off) "\n"                                              \
    "s_waitcnt lgkmcnt(0)\n"                                                                    \
    "v_readfirstlane_b32 %[st], v96\n"                                                          \
    "s_cmp_lt_u32 %[st], " STR(n) "\n"                                                          \
    "s_cbranch_scc1 " lbl "b\n"
#define LDS_ARRIVE(off)                                                                         \
    "s_mov_b64 %[sx], exec\n"                                                                   \
    "s_mov_b64 exec, 1\n"                                                                       \
    "v_mov_b32 v96, 1\n"                                                                        \
    "ds_add_u32 %[ca], v96 offset:" STR(off) "\n"                                               \
    "s_mov_b64 exec, %[sx]\n"
__device__ __forceinline__ void rendezvous8(unsigned ca) {
    int st;
    u64 sx;
    asm volatile(LDS_ARRIVE(0) LDS_WAIT(0, 8, "20")
                 : [st] "=&s"(st), [sx] "=&s"(sx) : [ca] "v"(ca) : "v96", "scc", "memory");
}
#define ATT_CLOB                                                                                \
    "v64", "v65", "v66", "v67", "v68", "v69", "v70", "v71", "v72", "v73", "v74", "v75", "v76",  \
    "v77", "v78", "v79", "v80", "v81", "v82", "v83", "v84", "v85", "v86", "v87", "v88", "v89",  \
    "v90", "v91", "v92", "v93", "v94", "v95", "v96", "v97", "v98", "v99", "v100", "v101",       \
    "v102", "v103", "v104", "v105", "v106", "v107", "v108", "v109", "v110", "v111", "v112",     \
    "v113", "v114", "v115", "v116", "v117", "v118", "v119", "v120", "v121", "v122", "v123",     \
    "v124", "v125", "v126", "v127", "v128", "v129", "v130", "v131"

#define ITER_STAMP
#define ITER_OUT
__device__ __forceinline__ f32x16 attn_pass(unsigned ka, unsigned va, unsigned vstep, unsigned ca,
                                            u32x4 q, float mgiven, int given) {
    f32x16 o = {0};
    int cnt, st;
    u64 sx;
    asm volatile(
        "ds_read_b128 v[104:107], %[ka]\n"
        "v_mov_b32 v116, %[mg]\n"
        "s_cmp_lg_u32 %[gv], 0\n"
        "s_waitcnt lgkmcnt(0)\n"
        "s_cbranch_scc1 11f\n"
        "v_mfma_f32_32x32x16_bf16 v[64:79], v[104:107], %[q], 0\n"
        "s_nop 11\n"
        "v_max3_f32 v96, v64, v65, v66\n"
        "v_max3_f32 v97, v67, v68, v69\n"
        "v_max3_f32 v98, v70, v71, v72\n"
        "v_max3_f32 v99, v73, v74, v75\n"
        "v_max3_f32 v96, v96, v76, v77\n"
        "v_max3_f32 v97, v97, v78, v79\n"
        "v_max3_f32 v96, v96, v98, v99\n"
        "v_max_f32 v96, v96, v97\n"
        "v_mov_b32 v97, v96\n"
        "s_nop 1\n"
        "v_permlane32_swap_b32 v96, v97\n"
        "s_nop 1\n"
        "v_max_f32 v116, v96, v97\n"
        "11:\n"
        "v_xor_b32 v116, 0x80000000, v116\n"
        MOVC(117) MOVC(118) MOVC(119) MOVC(120) MOVC(121) MOVC(122) MOVC(123) MOVC(124)
        MOVC(125) MOVC(126) MOVC(127) MOVC(128) MOVC(129) MOVC(130) MOVC(131)
        "v_mov_b32 v100, 0\n v_mov_b32 v101, 0\n v_mov_b32 v102, 0\n v_mov_b32 v103, 0\n"
        "s_movk_i32 %[cnt], 16\n"
        "s_setprio 2\n"
        "v_mfma_f32_32x32x16_bf16 v[64:79], v[104:107], %[q], v[116:131]\n"
        "ds_read_b128 v[104:107], %[ka] offset:1024\n"
        "ds_read_b128 v[112:115], %[va]\n"
        "ds_read_b128 v[108:111], %[va]\n"
        "s_nop 10\n"
        "12:\n"
        TILE_X(0) TILE_Y(1) TILE_X(2) TILE_Y(3)
        "v_add_u32 %[ka], 4096, %[ka]\n"
        "v_add_u32 %[va], %[vs], %[va]\n"
        "s_cmp_lg_u32 %[cnt], 16\n"
        "s_cbranch_scc1 14f\n"
        "s_waitcnt vmcnt(0)\n"
        LDS_ARRIVE(24)
        LDS_WAIT(24, 8, "13")
        "14:\n"
        "s_cmp_lg_u32 %[cnt], 10\n"
        "s_cbranch_scc1 15f\n"
        LDS_WAIT(4, 2, "17")
        "s_setprio 1\n"
        "15:\n"
        "s_cmp_lg_u32 %[cnt], 3\n"
        "s_cbranch_scc1 16f\n"
        "s_setprio 0\n"
        LDS_ARRIVE(8)
        "s_waitcnt lgkmcnt(0)\n"
        "16:\n"
        ITER_STAMP
        "s_sub_u32 %[cnt], %[cnt], 1\n"
        "s_cmp_lg_u32 %[cnt], 0\n"
        "s_cbranch_scc1 12b\n"
        "s_waitcnt lgkmcnt(0)\n"
        "v_mfma_f32_32x32x16_bf16 %[o], v[112:115], v[100:103], %[o]\n"
        "s_nop 15\n"
        : [o] "+v"(o), [ka] "+v"(ka), [va] "+v"(va), [cnt] "=&s"(cnt), [st] "=&s"(st), [sx] "=&s"(sx) ITER_OUT
        : [q] "v"(q), [vs] "v"(vstep), [ca] "v"(ca), [mg] "v"(mgiven), [gv] "s"(given)
        : ATT_CLOB, "scc", "memory", "s90", "s91", "s92", "s93");
    return o;
}

__device__ __forceinline__ float row_max(const char* kb, u32x4 q) {
    float m = -__builtin_inff();
    const f32x16 z = {0};
    for (int t = 0; t < 64; ++t) {
        const f32x16 s = mfma32(ld16(kb + t * 1024), q, z);
#pragma unroll
        for (int j = 0; j < 16; ++j) m = fmaxf(m, s[j]);
    }
    return fmaxf(m, __shfl_xor(m, 32));
}

#define GAP __builtin_amdgcn_sched_barrier(0); asm volatile("s_sleep 1"); __builtin_amdgcn_sched_barrier(0);
template <int T0, int T1, int T2, int T3>
__device__ __forceinline__ void conv_wave(__amdgpu_buffer_rsrc_t hr, const char* ul,
                                          __amdgpu_buffer_rsrc_t rs, int zoff, float dd, int lane) {
    constexpr int NF = T3 + 1;
    const int r = lane & 15, g = lane >> 4;
    const int ns = (SEQ - 1) - 16 * (g >> 1) - r + 8 * (g & 1);
    unsigned ap = (unsigned)(size_t)(LDSP const char*)ul + ((ns & 1) ? (U_RS + 2 * (ns - 1)) : 2 * ns) - 512 * (NF - 1);
    int b0 = 2 * (16 * r - 16 * (g >> 1) + 8 * (g & 1));
    f32x4 c0 = {0}, c1 = {0}, c2 = {0}, c3 = {0};
#pragma unroll 1
    for (int rho = 0; rho < 8; ++rho) {
        u32x4 A[NF], B[NF];
        int b1 = b0 + 512;
        asm volatile("" : "+v"(b1), "+v"(ap));
        B[0] = __builtin_amdgcn_raw_buffer_load_b128(hr, b0, 0, 0);
#pragma unroll
        for (int b = 1; b < NF; ++b) B[b] = __builtin_amdgcn_raw_buffer_load_b128(hr, b1 + 512 * (b - 1), 0, 0);
#pragma unroll
        for (int a = 0; a < NF; ++a) {
            volatile LDSP const unsigned* p = (volatile LDSP const unsigned*)(size_t)(ap + 512 * (NF - 1 - a));
            A[a] = (u32x4){p[0], p[1], p[2], p[3]};
        }
        __builtin_amdgcn_sched_barrier(0);
#pragma unroll
        for (int a = 0; a < NF; ++a) {
            c3 = mfma16(A[a], B[T3 - a], c3);
            GAP
            if (a <= T2) { c2 = mfma16(A[a], B[a <= T2 ? T2 - a : 0], c2); GAP }
            if (a <= T1) { c1 = mfma16(A[a], B[a <= T1 ? T1 - a : 0], c1); GAP }
            if (a <= T0) { c0 = mfma16(A[a], B[a <= T0 ? T0 - a : 0], c0); GAP }
        }
        b0 -= 64;
        ap -= 64;
    }
    const int ts[4] = {T0, T1, T2, T3};
    const f32x4 cs[4] = {c0, c1, c2, c3};
#pragma unroll
    for (int i = 0; i < 4; ++i) {
        const int s = 256 * ts[i] + 16 * r + 4 * g;
        const u32x2 uu = ld8(ul + 2 * ((SEQ - 4) - s));
        const u32x2 o = {pack2(fmaf(bfhi(uu[1]), dd, cs[i][0]), fmaf(bflo(uu[1]), dd, cs[i][1])),
                         pack2(fmaf(bfhi(uu[0]), dd, cs[i][2]), fmaf(bflo(uu[0]), dd, cs[i][3]))};
        __builtin_amdgcn_raw_buffer_store_b64(o, rs, zoff + 2 * s, 0, 16);
    }
}

template <bool WFIRST>
__device__ __forceinline__ void kv_proj(const char* xb, const char* wp, int g, int c, f32x4& a0, f32x4& a1) {
#pragma unroll
    for (int s = 0; s < 16; ++s) {
        const int xo = (s >> 2) * 256 + (((4 * (s & 3) + g) ^ c) << 4);
        const u32x4 fw = tr_frag(wp + s * 1024, 128), f0 = ld16(xb + xo), f1 = ld16(xb + xo + 16384);
        a0 = WFIRST ? mfma16(fw, f0, a0) : mfma16(f0, fw, a0);
        a1 = WFIRST ? mfma16(fw, f1, a1) : mfma16(f1, fw, a1);
    }
    __builtin_amdgcn_sched_group_barrier(0x100, 16, 0);
#pragma unroll
    for (int s = 0; s < 12; ++s) {
        __builtin_amdgcn_sched_group_barrier(0x008, 2, 0);
        __builtin_amdgcn_sched_group_barrier(0x100, 4, 0);
    }
    __builtin_amdgcn_sched_group_barrier(0x008, 8, 0);
}

__global__ void __launch_bounds__(768)
fused_kernel(const u16* __restrict__ x, const u16* __restrict__ wq, const u16* __restrict__ wk,
             const u16* __restrict__ wv, const u16* __restrict__ wo, const u16* __restrict__ bq,
             const u16* __restrict__ bk, const u16* __restrict__ bv, const u16* __restrict__ shk,
             const u16* __restrict__ ssmk, const u16* __restrict__ ssmd, u16* __restrict__ out,
             char* ws) {
    extern __shared__ __attribute__((aligned(16))) char smem[];
    const int lane = threadIdx.x & 63;
    const int w = __builtin_amdgcn_readfirstlane(threadIdx.x >> 6);
    const int blk = blockIdx.x;
    const int tg = (blk & 7) * 4 + (blk >> 6);
    const int hs = (blk >> 3) & 7;
    const int r = lane & 31, h = lane >> 5;
    const __amdgpu_buffer_rsrc_t rs = __builtin_amdgcn_make_buffer_rsrc(ws, 0, WS_END, 0x00020000);
    asm volatile("" ::"s"(x), "s"(wq), "s"(wk), "s"(wv), "s"(wo), "s"(bq), "s"(bk), "s"(bv), "s"(shk),
                 "s"(ssmk), "s"(ssmd), "s"(out), "s"(ws));

    const bool kvp = hs < 2;
    const int xt = hs & 1;
    if (w >= 8) {
        __builtin_amdgcn_s_setprio(3);
        const int wb4 = w - 8;
#pragma unroll
        for (int j = 0; j < 8; ++j) {
            const int row = 32 * xt + 4 * j + wb4;
            dma16(x + (size_t)(64 * tg + row) * DM + ((lane ^ (row & 15)) << 3), smem + L_X + row * 1024);
        }
        if (kvp) {
            const u16* src = (wb4 < 2 ? wk : wv) + (wb4 & 1) * 4096 + lane * 8;
#pragma unroll
            for (int j = 0; j < 8; ++j) dma16(src + j * 512, smem + L_WK + (wb4 * 8 + j) * 1024);
        }
    } else {
        if (kvp) barrier();
#pragma unroll
        for (int j = 0; j < 4; ++j) {
            const int row = 32 * (xt ^ 1) + 8 * j + w;
            dma16(x + (size_t)(64 * tg + row) * DM + ((lane ^ (row & 15)) << 3), smem + L_X + row * 1024);
        }
        const u16* src = wq + (size_t)(64 * w + (lane >> 3)) * DM + 64 * hs + (((lane & 7) ^ (((lane >> 4) & 1) << 2)) << 3);
#pragma unroll
        for (int j = 0; j < 8; ++j) dma16(src + (size_t)j * 8 * DM, smem + L_W + (8 * w + j) * 1024);
    }
    u64 tag;
    asm volatile("s_load_dwordx2 %0, %1, 0x0\n\ts_waitcnt lgkmcnt(0)" : "=s"(tag) : "s"(ws) : "memory");
    tag += 1;

    const int tq = (lane & 15) >> 2, tp = lane & 3;
    if (w < 8) {
        drain();
        barrier();
        const int i = w >> 2, hd = w & 3, kh = hd & 1;
        const char* xb = smem + L_X + (32 * i + r) * 1024 + kh * 512;
        const int chunk = 4 * (hd >> 1) + 2 * ((lane >> 4) & 1) + (tp >> 1);
        const char* wb = smem + L_W + kh * 32768 + (8 * h + tq) * 128 + ((chunk ^ ((tq >> 1) << 2)) << 4) + 8 * (tp & 1);
        u32x4 fa[16], fb[16];
#pragma unroll
        for (int s = 0; s < 16; ++s) {
            fa[s] = tr_frag(wb + s * 2048, 512);
            fb[s] = ld16(xb + (s >> 3) * 256 + (((2 * (s & 7) + h) ^ (r & 15)) << 4));
        }
        f32x16 acc = {0};
#pragma unroll
        for (int s = 0; s < 16; ++s) acc = mfma32(fa[s], fb[s], acc);
        proj_schedule();
        f32x4 keep[2];
        {
            f32x4* xp = reinterpret_cast<f32x4*>(smem + L_QX + (w ^ 1) * 2048) + lane;
#pragma unroll
            for (int g = 0; g < 2; ++g) {
                f32x4 lo = {acc[4 * g], acc[4 * g + 1], acc[4 * g + 2], acc[4 * g + 3]};
                f32x4 hi = {acc[8 + 4 * g], acc[9 + 4 * g], acc[10 + 4 * g], acc[11 + 4 * g]};
                xp[g * 64] = kh ? lo : hi;
                keep[g] = kh ? hi : lo;
            }
        }
        barrier();
        dma16_dev(ws + WS_KV + w * 2048 + lane * 16, smem + L_K + w * 1024);
        dma16_dev(ws + WS_KV + w * 2048 + 1024 + lane * 16, smem + L_V + w * 1024);
        rendezvous8((unsigned)(size_t)(LDSP char*)smem + L_CNT + 12);
#pragma unroll
        for (int j = 1; j < 4; ++j) {
            dma16_dev(ws + WS_KV + (w + 8 * j) * 2048 + lane * 16, smem + L_K + (w + 8 * j) * 1024);
            dma16_dev(ws + WS_KV + (w + 8 * j) * 2048 + 1024 + lane * 16, smem + L_V + (w + 8 * j) * 1024);
        }
        const unsigned base = (unsigned)(size_t)(LDSP char*)smem;
        u32x4 q;
        {
            f32x4 a, b;
            asm volatile("ds_read_b128 %0, %2\n\tds_read_b128 %1, %2 offset:1024\n\ts_waitcnt lgkmcnt(0)"
                         : "=&v"(a), "=&v"(b) : "v"(base + L_QX + w * 2048 + lane * 16) : "memory");
            a = (a + keep[0]) * QSCALE;
            b = (b + keep[1]) * QSCALE;
            q = (u32x4){pack2(a[0], a[1]), pack2(a[2], a[3]), pack2(b[0], b[1]), pack2(b[2], b[3])};
        }
        const unsigned ka = base + L_K + lane * 16;
        const unsigned va = base + (r < 16 ? L_V + h * 256 + r * 16
                                           : L_CONST + (r == 16 ? 0 : ((r >= 20 && r < 28) ? 64 : 16)));
        const unsigned vstep = r < 16 ? 4096 : 0;
        asm volatile("s_waitcnt vmcnt(6)" ::: "memory");
        rendezvous8(base + L_CNT);
        float m = 0.f;
        f32x16 o;
        float l;
#pragma unroll 1
        for (int given = 0; given < 2; ++given) {
            o = attn_pass(ka, va, vstep, base + L_CNT, q, m, given);
            l = __shfl(o[8], r);
            float mag = 0.f;
#pragma unroll
            for (int j = 0; j < 8; ++j) mag += fabsf(o[j]);
            if (!__any(!(l < 1.0e18f) || !(mag < 3.0e38f))) break;
            m = row_max(smem + L_K + lane * 16, q);
        }
        const float inv = 1.0f / l;
        u32x4 of;
#pragma unroll
        for (int j = 0; j < 4; ++j) of[j] = pack2(o[2 * j] * inv, o[2 * j + 1] * inv);
        __builtin_amdgcn_raw_buffer_store_b128(
            of, rs, WS_OF + ((2 * tg + i) * 32 + 4 * hs + hd) * 1024 + lane * 16, 0, 16);
        publish(ws, WS_OFL + ((tg * 8 + hs) * 8 + w) * FL, tag, lane);
        return;
    }

    const int wb4 = w - 8;
    drain();
    if (kvp) barrier();
    if (wb4 < 2) {
        const int tt = 2 * tg + hs;
        if (kvp) {
            const int c = lane & 15, g = lane >> 4;
            const char* xb = smem + L_X + (32 * hs + c) * 1024;
            const char* wp = smem + (wb4 == 0 ? L_WK : L_WV) + (8 * g + tq) * 32 + 8 * tp;
            f32x4 a0 = {0}, a1 = {0};
            if (wb4 == 0) kv_proj<true>(xb, wp, g, c, a0, a1);
            else kv_proj<false>(xb, wp, g, c, a0, a1);
            const int off = WS_KV + tt * 2048 + wb4 * 1024 + (g & 1) * (wb4 ? 256 : 512) + c * 16 + (g >> 1) * 8;
            __builtin_amdgcn_raw_buffer_store_b64((u32x2){pack2(a0[0], a0[1]), pack2(a0[2], a0[3])}, rs, off, 0, 16);
            __builtin_amdgcn_raw_buffer_store_b64((u32x2){pack2(a1[0], a1[1]), pack2(a1[2], a1[3])}, rs,
                                                  off + (wb4 ? 512 : 256), 0, 16);
            drain();
            if (lane < 8)
                __hip_atomic_store((GLBP u64*)(ws + WS_KVF + lane * KVF_REP + (tt * 2 + wb4) * FL), tag,
                                   __ATOMIC_RELAXED, __HIP_MEMORY_SCOPE_AGENT);
        }
        barrier();
        if (wb4 == 0) {
            if (lane < 27) {
                const int b = lane / 3, p = lane - 3 * b;
                const unsigned v = p == 0 ? 0x3f803f80u : 0u;
                *reinterpret_cast<u32x4*>(smem + L_CONST + b * 512 + (p == 2 ? 64 : 16 * p)) = (u32x4){v, v, v, v};
            } else if (lane < 31) {
                *reinterpret_cast<u32x4*>(smem + L_CNT + (lane - 27) * 16) = (u32x4){0, 0, 0, 0};
            }
        }
        wait_flags<2>(ws, WS_KVF + (blk & 7) * KVF_REP + (wb4 * 64 + lane) * FL, true, tag);
        barrier();
        {
            lds_wait((LDSP unsigned*)(smem + L_CNT), 8);
            const char* src = ws + WS_KV + 32 * 2048 + wb4 * 1024 + lane * 16;
            char* dst = smem + (wb4 ? L_V : L_K) + 32 * 1024;
#pragma unroll 8
            for (int t = 0; t < 32; ++t) dma16_dev(src + t * 2048, dst + t * 1024);
            drain();
            lds_arrive((LDSP unsigned*)(smem + L_CNT + 4), lane);
        }
    } else {
        barrier();
        const int s0 = 64 * tg + 8 * ((lane >> 4) + 4 * (wb4 & 1));
        const int d0 = 64 * hs + 4 * (lane & 15);
        const __amdgpu_buffer_rsrc_t kr_ = __builtin_amdgcn_make_buffer_rsrc((void*)bk, 0, SEQ * DM * 2, 0x00020000);
        const __amdgpu_buffer_rsrc_t vr_ = __builtin_amdgcn_make_buffer_rsrc((void*)bv, 0, SEQ * DM * 2, 0x00020000);
        const int o0 = (s0 * DM + d0) * 2;
        int om = o0 - 3072;
        asm volatile("" : "+v"(om));
        u32x2 kr[11], vr[8];
#pragma unroll
        for (int j = 0; j < 3; ++j) kr[j] = __builtin_amdgcn_raw_buffer_load_b64(kr_, om + 1024 * j, 0, 0);
#pragma unroll
        for (int j = 0; j < 8; ++j) {
            kr[3 + j] = __builtin_amdgcn_raw_buffer_load_b64(kr_, o0 + 1024 * (j & 3), 4096 * (j >> 2), 0);
            vr[j] = __builtin_amdgcn_raw_buffer_load_b64(vr_, o0 + 1024 * (j & 3), 4096 * (j >> 2), 0);
        }
        const u32x4 sk0 = ld16(shk + d0 * 4), sk1 = ld16(shk + d0 * 4 + 8);
#pragma unroll
        for (int e = 0; e < 4; ++e) {
            const unsigned w01 = (e < 2 ? sk0 : sk1)[(e & 1) * 2], w23 = (e < 2 ? sk0 : sk1)[(e & 1) * 2 + 1];
            const float w0 = bflo(w01), w1 = bfhi(w01), w2 = bflo(w23), w3 = bfhi(w23);
            float kk[11], u[8];
#pragma unroll
            for (int j = 0; j < 11; ++j) kk[j] = (e & 1) ? bfhi(kr[j][e >> 1]) : bflo(kr[j][e >> 1]);
#pragma unroll
            for (int j = 0; j < 8; ++j) {
                const float a = fmaf(kk[j + 3], w0, fmaf(kk[j + 2], w1, fmaf(kk[j + 1], w2, kk[j] * w3)));
                u[j] = a * ((e & 1) ? bfhi(vr[j][e >> 1]) : bflo(vr[j][e >> 1]));
            }
            const u32x4 o = {pack2(u[7], u[6]), pack2(u[5], u[4]), pack2(u[3], u[2]), pack2(u[1], u[0])};
            __builtin_amdgcn_raw_buffer_store_b128(o, rs, WS_UR + ((d0 + e) * SEQ + (SEQ - 8) - s0) * 2, 0, 16);
        }
        publish(ws, WS_URF + ((hs * 32 + tg) * 2 + (wb4 & 1)) * FL, tag, lane);
        barrier();
    }

    {
        const int ch = wb4 >> 1, sh = wb4 & 1;
        const int d = 2 * blk + ch;
        const float dd = bf2f(ssmd[d]);
        wait_flags<24>(ws, WS_URF + ((d >> 6) * 64 + lane) * FL, true, tag);
        char* ul = smem + L_U + ch * 2 * U_RS;
        u32x4 uv[4];
#pragma unroll
        for (int j = 0; j < 4; ++j)
            uv[j] = __builtin_amdgcn_raw_buffer_load_b128(rs, WS_UR + d * 4096 + j * 1024 + lane * 16 + 2 * sh, 0, 16);
        if (sh && lane == 63) uv[3][3] &= 0xffffu;
        lds_wait((LDSP unsigned*)(smem + L_CNT), 8);
#pragma unroll
        for (int j = 0; j < 4; ++j)
            *reinterpret_cast<u32x4*>(ul + sh * U_RS + j * 1024 + lane * 16) = uv[j];
        if (lane < 2) *reinterpret_cast<u32x4*>(ul + sh * U_RS + 4096 + lane * 16) = (u32x4){0, 0, 0, 0};
        LDSP unsigned* pc = (LDSP unsigned*)(smem + L_CNT + 16 + 4 * ch);
        lds_arrive(pc, lane);
        lds_wait(pc, 2);
        const __amdgpu_buffer_rsrc_t hk = __builtin_amdgcn_make_buffer_rsrc((void*)(ssmk + (size_t)d * SEQ), 0, SEQ * 2, 0x00020000);
        if (sh) conv_wave<1, 2, 4, 7>(hk, ul, rs, WS_ZT + d * 4096, dd, lane);
        else conv_wave<0, 3, 5, 6>(hk, ul, rs, WS_ZT + d * 4096, dd, lane);
        publish(ws, WS_ZF + (d * 2 + sh) * FL, tag, lane);
    }

    const __amdgpu_buffer_rsrc_t ro = __builtin_amdgcn_make_buffer_rsrc(out, 0, SEQ * 1024 * 2, 0x00020000);
    if (wb4 < 2) {
        const int c8 = lane & 7;
        const int s0 = 64 * tg + 4 * ((lane >> 3) + 8 * wb4);
        const int d0 = 64 * hs + 8 * c8;
        u32x4 qr[4];
#pragma unroll
        for (int j = 0; j < 4; ++j) qr[j] = ld16(bq + (size_t)(s0 + j) * DM + d0);
        const int sh = (0x96 >> (tg >> 2)) & 1;
        wait_flags<24>(ws, WS_ZF + ((64 * hs + lane) * 2 + sh) * FL, true, tag);
        u32x2 z[8];
#pragma unroll
        for (int e = 0; e < 8; ++e)
            z[e] = __builtin_amdgcn_raw_buffer_load_b64(rs, WS_ZT + ((d0 + e) * SEQ + s0) * 2, 0, 16);
#pragma unroll
        for (int i = 0; i < 4; ++i) {
            u32x4 o;
#pragma unroll
            for (int j = 0; j < 4; ++j) {
                const unsigned za = z[2 * j][i >> 1], zb = z[2 * j + 1][i >> 1];
                const float a = (i & 1) ? bfhi(za) : bflo(za);
                const float b = (i & 1) ? bfhi(zb) : bflo(zb);
                o[j] = pack2(bflo(qr[i][j]) * a, bfhi(qr[i][j]) * b);
            }
            __builtin_amdgcn_raw_buffer_store_b128(o, ro, ((s0 + i) * 1024 + 512 + d0) * 2, 0, OUT_AUX);
        }
    } else if (blk == 0 && wb4 == 2) {
#pragma unroll 1
        for (int j = 0; j < 8; ++j) wait_flags<24>(ws, WS_URF + (j * 64 + lane) * FL, true, tag);
        if (lane == 0) __hip_atomic_store((GLBP u64*)(ws + WS_EPOCH), tag, __ATOMIC_RELAXED, __HIP_MEMORY_SCOPE_AGENT);
    }

    u32x4 fe[8], fo[8];
    {
        const __amdgpu_buffer_rsrc_t wr = __builtin_amdgcn_make_buffer_rsrc((void*)wo, 0, DM * DM * 2, 0x00020000);
        const int vo = (4 * h * DM + 2 * (r ^ (8 * wb4))) * 2;
        const int so = (128 * wb4 * DM + 64 * hs) * 2;
        unsigned wd[8][8];
#pragma unroll
        for (int s = 0; s < 8; ++s)
#pragma unroll
            for (int j = 0; j < 8; ++j)
                wd[s][j] = __builtin_amdgcn_raw_buffer_load_b32(wr, vo + (j & 3) * 1024, so + (16 * s + 8 * (j >> 2)) * 1024, 0);
        __builtin_amdgcn_sched_barrier(0);
#pragma unroll
        for (int s = 0; s < 8; ++s) {
#pragma unroll
            for (int j = 0; j < 4; ++j) {
                fe[s][j] = __builtin_amdgcn_perm(wd[s][2 * j + 1], wd[s][2 * j], 0x05040100u);
                fo[s][j] = __builtin_amdgcn_perm(wd[s][2 * j + 1], wd[s][2 * j], 0x07060302u);
            }
            asm volatile("" : "+v"(fe[s]), "+v"(fo[s]));
        }
    }
    LDSP unsigned* cnt = (LDSP unsigned*)(smem + L_CNT + 32);
    lds_arrive(cnt, lane);
    while (__hip_atomic_load((LDSP unsigned*)(smem + L_CNT + 8), __ATOMIC_RELAXED, __HIP_MEMORY_SCOPE_WORKGROUP) < 4u)
        __builtin_amdgcn_s_sleep(8);
#pragma unroll 1
    for (int i = 0; i < 2; ++i) {
        wait_flags<1>(ws, WS_OFL + ((tg * 8 + 2 * wb4 + (lane >> 2)) * 8 + 4 * i + (lane & 3)) * FL, lane < 8, tag);
        u32x4 of[8];
#pragma unroll
        for (int s = 0; s < 8; ++s)
            of[s] = __builtin_amdgcn_raw_buffer_load_b128(
                rs, lane * 16 + (s & 3) * 1024, WS_OF + ((2 * tg + i) * 32 + 8 * wb4 + (s & 4)) * 1024, 16);
        f32x16 ae = {0}, ao = {0};
#pragma unroll
        for (int s = 0; s < 8; ++s) {
            ae = mfma32(fe[s], of[s], ae);
            ao = mfma32(fo[s], of[s], ao);
        }
        lds_wait(cnt, 4 + 8 * i);
#pragma unroll
        for (int g = 1; g < 4; ++g) {
            f32x4* p = reinterpret_cast<f32x4*>(smem + L_RED + ((g ^ wb4) * 3 + g - 1) * 2048) + lane;
            p[0] = (f32x4){ae[4 * g], ae[4 * g + 1], ae[4 * g + 2], ae[4 * g + 3]};
            p[64] = (f32x4){ao[4 * g], ao[4 * g + 1], ao[4 * g + 2], ao[4 * g + 3]};
        }
        lds_arrive(cnt, lane);
        lds_wait(cnt, 8 + 8 * i);
        f32x4 e = {ae[0], ae[1], ae[2], ae[3]}, o = {ao[0], ao[1], ao[2], ao[3]};
        const f32x4* p = reinterpret_cast<const f32x4*>(smem + L_RED + wb4 * 3 * 2048) + lane;
#pragma unroll
        for (int g = 0; g < 3; ++g) {
            e += p[g * 128];
            o += p[g * 128 + 64];
        }
        const u32x4 res = {pack2(e[0], o[0]), pack2(e[1], o[1]), pack2(e[2], o[2]), pack2(e[3], o[3])};
        __builtin_amdgcn_raw_buffer_store_b128(res, ro, ((64 * tg + 32 * i + r) * 1024 + 64 * hs + 16 * wb4 + 8 * h) * 2, 0, OUT_AUX);
        lds_arrive(cnt, lane);
    }
}

extern "C" void kernel_launch(void* const* d_in, const int* in_sizes, int n_in, void* d_out,
                              int out_size, void* d_ws, size_t ws_size, hipStream_t stream) {
    (void)hipFuncSetAttribute((const void*)fused_kernel, hipFuncAttributeMaxDynamicSharedMemorySize, LDS_BYTES);
    fused_kernel<<<256, 768, LDS_BYTES, stream>>>(
        (const u16*)d_in[0], (const u16*)d_in[1], (const u16*)d_in[2], (const u16*)d_in[3],
        (const u16*)d_in[4], (const u16*)d_in[5], (const u16*)d_in[6], (const u16*)d_in[7],
        (const u16*)d_in[8], (const u16*)d_in[9], (const u16*)d_in[10], (u16*)d_out, (char*)d_ws);
}
```

```cpp
#include <hip/hip_runtime.h>
#include <hip/hip_bf16.h>
#define CSP 3

typedef __attribute__((ext_vector_type(8))) __bf16 bf16x8;
typedef __attribute__((ext_vector_type(2))) __bf16 bf16x2;
typedef __attribute__((ext_vector_type(16))) float f32x16;
typedef __attribute__((ext_vector_type(4))) float f32x4;
typedef __attribute__((ext_vector_type(2))) float f32x2;
typedef __attribute__((ext_vector_type(4))) unsigned u32x4;
typedef __attribute__((ext_vector_type(2))) unsigned u32x2;
typedef __attribute__((ext_vector_type(4))) short s16x4;
typedef unsigned long long u64;
typedef unsigned short u16;
#define DG_RT(v) asm volatile("s_memrealtime %0\n\ts_waitcnt lgkmcnt(0)" : "=s"(v) :: "memory")

#define LDSP __attribute__((address_space(3)))
#define GLBP __attribute__((address_space(1)))

constexpr int SEQ = 2048;
constexpr int DM = 512;

constexpr int FL = 128;
constexpr int WS_EPOCH = 0;
constexpr int KVF_REP = 128 * FL + 4096;
constexpr int WS_KVF = 4096;
constexpr int WS_URF = WS_KVF + 8 * KVF_REP;
constexpr int WS_ZF = WS_URF + 512 * FL;
constexpr int WS_OFL = WS_ZF + 1024 * FL;
constexpr int WS_KV = WS_OFL + 2048 * FL;
constexpr int WS_UR = WS_KV + 131072;
constexpr int WS_ZT = WS_UR + 2097152 + 4096;
constexpr int WS_OF = WS_ZT + 2097152;
constexpr int WS_END = WS_OF + 2097152;

constexpr int L_X = 0;
constexpr int L_W = 65536;
constexpr int L_WK = 131072;
constexpr int L_WV = 147456;
constexpr int L_K = 0;
constexpr int L_V = 65536;
constexpr int L_CONST = 131072;
constexpr int L_CNT = L_CONST + 9 * 512;
constexpr int L_U = L_CNT + 64;
constexpr int U_RS = 4160;
constexpr int L_RED = L_U;
constexpr int L_QX = 147456;
constexpr int LDS_BYTES = 163840;

constexpr float QSCALE = 0.25f * 1.4426950408889634f;
constexpr unsigned SPIN_CAP = 1u << 18;

__device__ __forceinline__ float bf2f(u16 v) { return __uint_as_float(((unsigned)v) << 16); }
__device__ __forceinline__ float bflo(unsigned v) { return __uint_as_float(v << 16); }
__device__ __forceinline__ float bfhi(unsigned v) { return __uint_as_float(v & 0xffff0000u); }
__device__ __forceinline__ unsigned pack2(float lo, float hi) {
    f32x2 f = {lo, hi};
    bf16x2 b = __builtin_convertvector(f, bf16x2);
    return __builtin_bit_cast(unsigned, b);
}
__device__ __forceinline__ float rbf(float x) { return (float)(__bf16)x; }
__device__ __forceinline__ bf16x8 as_bf(u32x4 v) { return __builtin_bit_cast(bf16x8, v); }
__device__ __forceinline__ f32x16 mfma32(u32x4 a, u32x4 b, f32x16 c) {
    return __builtin_amdgcn_mfma_f32_32x32x16_bf16(as_bf(a), as_bf(b), c, 0, 0, 0);
}
__device__ __forceinline__ f32x4 mfma16(u32x4 a, u32x4 b, f32x4 c) {
    return __builtin_amdgcn_mfma_f32_16x16x32_bf16(as_bf(a), as_bf(b), c, 0, 0, 0);
}
__device__ __forceinline__ u32x4 ld16(const void* p) { return *reinterpret_cast<const u32x4*>(p); }
__device__ __forceinline__ u32x2 ld8(const void* p) { return *reinterpret_cast<const u32x2*>(p); }
__device__ __forceinline__ u32x4 tr_frag(const char* p, int second) {
    const s16x4 a = __builtin_amdgcn_ds_read_tr16_b64_v4i16((LDSP s16x4*)p);
    const s16x4 b = __builtin_amdgcn_ds_read_tr16_b64_v4i16((LDSP s16x4*)(p + second));
    const u32x2 a2 = __builtin_bit_cast(u32x2, a), b2 = __builtin_bit_cast(u32x2, b);
    return (u32x4){a2[0], a2[1], b2[0], b2[1]};
}
__device__ __forceinline__ void dma16(const void* src, char* dst) {
    __builtin_amdgcn_global_load_lds(src, (LDSP void*)dst, 16, 0, 0);
}
__device__ __forceinline__ void dma16_dev(const void* src, char* dst) {
    __builtin_amdgcn_global_load_lds(src, (LDSP void*)dst, 16, 0, 16);
}
__device__ __forceinline__ void barrier() {
    asm volatile("s_waitcnt lgkmcnt(0)\n\ts_barrier" ::: "memory");
}
__device__ __forceinline__ void proj_schedule() {
    __builtin_amdgcn_sched_group_barrier(0x100, 12, 0);
#pragma unroll
    for (int s = 0; s < 12; ++s) {
        __builtin_amdgcn_sched_group_barrier(0x008, 1, 0);
        __builtin_amdgcn_sched_group_barrier(0x100, 3, 0);
    }
    __builtin_amdgcn_sched_group_barrier(0x008, 4, 0);
}
__device__ __forceinline__ void drain() { asm volatile("s_waitcnt vmcnt(0)" ::: "memory"); }

__device__ __forceinline__ void lds_arrive(LDSP unsigned* c, int lane) {
    asm volatile("s_waitcnt lgkmcnt(0)" ::: "memory");
    if (lane == 0) __hip_atomic_fetch_add(c, 1u, __ATOMIC_RELAXED, __HIP_MEMORY_SCOPE_WORKGROUP);
}
__device__ __forceinline__ void lds_wait(LDSP unsigned* c, unsigned n) {
    while (__hip_atomic_load(c, __ATOMIC_RELAXED, __HIP_MEMORY_SCOPE_WORKGROUP) < n) {}
    asm volatile("" ::: "memory");
}

__device__ __forceinline__ void publish(char* ws, int off, u64 tag, int lane) {
    drain();
    if (lane == 0) __hip_atomic_store((GLBP u64*)(ws + off), tag, __ATOMIC_RELAXED, __HIP_MEMORY_SCOPE_AGENT);
}
template <int SLEEP>
__device__ __forceinline__ void wait_flags(char* ws, int off, bool active, u64 tag) {
#pragma unroll 1
    for (unsigned spin = 0; spin < SPIN_CAP; ++spin) {
        u64 v = tag;
        if (active) v = __hip_atomic_load((GLBP u64*)(ws + off), __ATOMIC_RELAXED, __HIP_MEMORY_SCOPE_AGENT);
        if (__all(v == tag)) break;
        __builtin_amdgcn_s_sleep(SLEEP);
    }
    asm volatile("" ::: "memory");
}

#define STR2(x) #x
#define STR(x) STR2(x)
#define EX(r) "v_exp_f32 v" STR(r) ", v" STR(r) "\n"
#define CV(d, a, b) "v_cvt_pk_bf16_f32 v" STR(d) ", v" STR(a) ", v" STR(b) "\n"
#define TILE(x0, x1, x2, x3, x4, x5, x6, x7, x8, x9, x10, x11, x12, x13, x14, x15, NEXT, u)     \
    "s_waitcnt lgkmcnt(2)\n"                                                                    \
    "v_mfma_f32_32x32x16_bf16 " NEXT ", v[104:107], %[q], v[116:131]\n"                         \
    "ds_read_b128 v[104:107], %[ka] offset:" STR((u + 2) * 1024) "\n"                           \
    EX(x0) EX(x1) EX(x2) EX(x3) CV(96, x0, x1) EX(x4) EX(x5) CV(97, x2, x3)                     \
    "s_waitcnt lgkmcnt(2)\n"                                                                    \
    "v_mfma_f32_32x32x16_bf16 %[o], v[112:115], v[100:103], %[o]\n"                             \
    "ds_read_b128 v[112:115], %[va] offset:" STR((2 * u + 1) * 512) "\n"                        \
    EX(x6) EX(x7) CV(98, x4, x5) EX(x8) CV(99, x6, x7) EX(x9) EX(x10) EX(x11)                   \
    "s_waitcnt lgkmcnt(2)\n"                                                                    \
    "v_mfma_f32_32x32x16_bf16 %[o], v[108:111], v[96:99], %[o]\n"                               \
    "ds_read_b128 v[108:111], %[va] offset:" STR((2 * u + 2) * 512) "\n"                        \
    CV(100, x8, x9) EX(x12) EX(x13) CV(101, x10, x11) EX(x14) EX(x15) CV(102, x12, x13)         \
    "s_nop 0\n" CV(103, x14, x15)
#define TILE_X(u) TILE(64, 65, 66, 67, 68, 69, 70, 71, 72, 73, 74, 75, 76, 77, 78, 79, "v[80:95]", u)
#define TILE_Y(u) TILE(80, 81, 82, 83, 84, 85, 86, 87, 88, 89, 90, 91, 92, 93, 94, 95, "v[64:79]", u)
#define MOVC(r) "v_mov_b32 v" STR(r) ", v116\n"
#define LDS_WAIT(off, n, lbl)                                                                   \
    lbl ":\n"                                                                                   \
    "ds_read_b32 v96, %[ca] offset:" STR(off) "\n"                                              \
    "s_waitcnt lgkmcnt(0)\n"                                                                    \
    "v_readfirstlane_b32 %[st], v96\n"                                                          \
    "s_cmp_lt_u32 %[st], " STR(n) "\n"                                                          \
    "s_cbranch_scc1 " lbl "b\n"
#define LDS_ARRIVE(off)                                                                         \
    "s_mov_b64 %[sx], exec\n"                                                                   \
    "s_mov_b64 exec, 1\n"                                                                       \
    "v_mov_b32 v96, 1\n"                                                                        \
    "ds_add_u32 %[ca], v96 offset:" STR(off) "\n"                                               \
    "s_mov_b64 exec, %[sx]\n"
__device__ __forceinline__ void rendezvous8(unsigned ca) {
    int st;
    u64 sx;
    asm volatile(LDS_ARRIVE(0) LDS_WAIT(0, 8, "20")
                 : [st] "=&s"(st), [sx] "=&s"(sx) : [ca] "v"(ca) : "v96", "scc", "memory");
}
#define ATT_CLOB                                                                                \
    "v64", "v65", "v66", "v67", "v68", "v69", "v70", "v71", "v72", "v73", "v74", "v75", "v76",  \
    "v77", "v78", "v79", "v80", "v81", "v82", "v83", "v84", "v85", "v86", "v87", "v88", "v89",  \
    "v90", "v91", "v92", "v93", "v94", "v95", "v96", "v97", "v98", "v99", "v100", "v101",       \
    "v102", "v103", "v104", "v105", "v106", "v107", "v108", "v109", "v110", "v111", "v112",     \
    "v113", "v114", "v115", "v116", "v117", "v118", "v119", "v120", "v121", "v122", "v123",     \
    "v124", "v125", "v126", "v127", "v128", "v129", "v130", "v131"

#define ITER_STAMP
#define ITER_OUT
__device__ __forceinline__ f32x16 attn_pass(unsigned ka, unsigned va, unsigned vstep, unsigned ca,
                                            u32x4 q, float mgiven, int given) {
    f32x16 o = {0};
    int cnt, st;
    u64 sx;
    asm volatile(
        "ds_read_b128 v[104:107], %[ka]\n"
        "v_mov_b32 v116, %[mg]\n"
        "s_cmp_lg_u32 %[gv], 0\n"
        "s_waitcnt lgkmcnt(0)\n"
        "s_cbranch_scc1 11f\n"
        "v_mfma_f32_32x32x16_bf16 v[64:79], v[104:107], %[q], 0\n"
        "s_nop 11\n"
        "v_max3_f32 v96, v64, v65, v66\n"
        "v_max3_f32 v97, v67, v68, v69\n"
        "v_max3_f32 v98, v70, v71, v72\n"
        "v_max3_f32 v99, v73, v74, v75\n"
        "v_max3_f32 v96, v96, v76, v77\n"
        "v_max3_f32 v97, v97, v78, v79\n"
        "v_max3_f32 v96, v96, v98, v99\n"
        "v_max_f32 v96, v96, v97\n"
        "v_mov_b32 v97, v96\n"
        "s_nop 1\n"
        "v_permlane32_swap_b32 v96, v97\n"
        "s_nop 1\n"
        "v_max_f32 v116, v96, v97\n"
        "11:\n"
        "v_xor_b32 v116, 0x80000000, v116\n"
        MOVC(117) MOVC(118) MOVC(119) MOVC(120) MOVC(121) MOVC(122) MOVC(123) MOVC(124)
        MOVC(125) MOVC(126) MOVC(127) MOVC(128) MOVC(129) MOVC(130) MOVC(131)
        "v_mov_b32 v100, 0\n v_mov_b32 v101, 0\n v_mov_b32 v102, 0\n v_mov_b32 v103, 0\n"
        "s_movk_i32 %[cnt], 16\n"
        "s_setprio 2\n"
        "v_mfma_f32_32x32x16_bf16 v[64:79], v[104:107], %[q], v[116:131]\n"
        "ds_read_b128 v[104:107], %[ka] offset:1024\n"
        "ds_read_b128 v[112:115], %[va]\n"
        "ds_read_b128 v[108:111], %[va]\n"
        "s_nop 10\n"
        "12:\n"
        TILE_X(0) TILE_Y(1) TILE_X(2) TILE_Y(3)
        "v_add_u32 %[ka], 4096, %[ka]\n"
        "v_add_u32 %[va], %[vs], %[va]\n"
        "s_cmp_lg_u32 %[cnt], 16\n"
        "s_cbranch_scc1 14f\n"
        "s_waitcnt vmcnt(0)\n"
        LDS_ARRIVE(24)
        LDS_WAIT(24, 8, "13")
        "14:\n"
        "s_cmp_lg_u32 %[cnt], 10\n"
        "s_cbranch_scc1 15f\n"
        LDS_WAIT(4, 2, "17")
        "s_setprio 1\n"
        "15:\n"
        "s_cmp_lg_u32 %[cnt], 3\n"
        "s_cbranch_scc1 16f\n"
        "s_setprio 0\n"
        LDS_ARRIVE(8)
        "s_waitcnt lgkmcnt(0)\n"
        "16:\n"
        ITER_STAMP
        "s_sub_u32 %[cnt], %[cnt], 1\n"
        "s_cmp_lg_u32 %[cnt], 0\n"
        "s_cbranch_scc1 12b\n"
        "s_waitcnt lgkmcnt(0)\n"
        "v_mfma_f32_32x32x16_bf16 %[o], v[112:115], v[100:103], %[o]\n"
        "s_nop 15\n"
        : [o] "+v"(o), [ka] "+v"(ka), [va] "+v"(va), [cnt] "=&s"(cnt), [st] "=&s"(st), [sx] "=&s"(sx) ITER_OUT
        : [q] "v"(q), [vs] "v"(vstep), [ca] "v"(ca), [mg] "v"(mgiven), [gv] "s"(given)
        : ATT_CLOB, "scc", "memory", "s90", "s91", "s92", "s93");
    return o;
}

__device__ __forceinline__ float row_max(const char* kb, u32x4 q) {
    float m = -__builtin_inff();
    const f32x16 z = {0};
    for (int t = 0; t < 64; ++t) {
        const f32x16 s = mfma32(ld16(kb + t * 1024), q, z);
#pragma unroll
        for (int j = 0; j < 16; ++j) m = fmaxf(m, s[j]);
    }
    return fmaxf(m, __shfl_xor(m, 32));
}

#define GAP __builtin_amdgcn_sched_barrier(0); asm volatile("s_sleep 1"); __builtin_amdgcn_sched_barrier(0);
template <int T0, int T1, int T2, int T3>
__device__ __forceinline__ void conv_wave(__amdgpu_buffer_rsrc_t hr, const char* ul,
                                          __amdgpu_buffer_rsrc_t rs, int zoff, float dd, int lane) {
    constexpr int NF = T3 + 1;
    const int r = lane & 15, g = lane >> 4;
    const int ns = (SEQ - 1) - 16 * (g >> 1) - r + 8 * (g & 1);
    unsigned ap = (unsigned)(size_t)(LDSP const char*)ul + ((ns & 1) ? (U_RS + 2 * (ns - 1)) : 2 * ns) - 512 * (NF - 1);
    int b0 = 2 * (16 * r - 16 * (g >> 1) + 8 * (g & 1));
    f32x4 c0 = {0}, c1 = {0}, c2 = {0}, c3 = {0};
#pragma unroll 1
    for (int rho = 0; rho < 8; ++rho) {
        u32x4 A[NF], B[NF];
        int b1 = b0 + 512;
        asm volatile("" : "+v"(b1), "+v"(ap));
        B[0] = __builtin_amdgcn_raw_buffer_load_b128(hr, b0, 0, 0);
#pragma unroll
        for (int b = 1; b < NF; ++b) B[b] = __builtin_amdgcn_raw_buffer_load_b128(hr, b1 + 512 * (b - 1), 0, 0);
#pragma unroll
        for (int a = 0; a < NF; ++a) {
            volatile LDSP const unsigned* p = (volatile LDSP const unsigned*)(size_t)(ap + 512 * (NF - 1 - a));
            A[a] = (u32x4){p[0], p[1], p[2], p[3]};
        }
        __builtin_amdgcn_sched_barrier(0);
#pragma unroll
        for (int a = 0; a < NF; ++a) {
            c3 = mfma16(A[a], B[T3 - a], c3);
            GAP
            if (a <= T2) { c2 = mfma16(A[a], B[a <= T2 ? T2 - a : 0], c2); GAP }
            if (a <= T1) { c1 = mfma16(A[a], B[a <= T1 ? T1 - a : 0], c1); GAP }
            if (a <= T0) { c0 = mfma16(A[a], B[a <= T0 ? T0 - a : 0], c0); GAP }
        }
        b0 -= 64;
        ap -= 64;
    }
    const int ts[4] = {T0, T1, T2, T3};
    const f32x4 cs[4] = {c0, c1, c2, c3};
#pragma unroll
    for (int i = 0; i < 4; ++i) {
        const int s = 256 * ts[i] + 16 * r + 4 * g;
        const u32x2 uu = ld8(ul + 2 * ((SEQ - 4) - s));
        const u32x2 o = {pack2(fmaf(bfhi(uu[1]), dd, cs[i][0]), fmaf(bflo(uu[1]), dd, cs[i][1])),
                         pack2(fmaf(bfhi(uu[0]), dd, cs[i][2]), fmaf(bflo(uu[0]), dd, cs[i][3]))};
        __builtin_amdgcn_raw_buffer_store_b64(o, rs, zoff + 2 * s, 0, 16);
    }
}

template <bool WFIRST>
__device__ __forceinline__ void kv_proj(const char* xb, const char* wp, int g, int c, f32x4& a0, f32x4& a1) {
#pragma unroll
    for (int s = 0; s < 16; ++s) {
        const int xo = (s >> 2) * 256 + (((4 * (s & 3) + g) ^ c) << 4);
        const u32x4 fw = tr_frag(wp + s * 1024, 128), f0 = ld16(xb + xo), f1 = ld16(xb + xo + 16384);
        a0 = WFIRST ? mfma16(fw, f0, a0) : mfma16(f0, fw, a0);
        a1 = WFIRST ? mfma16(fw, f1, a1) : mfma16(f1, fw, a1);
    }
    __builtin_amdgcn_sched_group_barrier(0x100, 16, 0);
#pragma unroll
    for (int s = 0; s < 12; ++s) {
        __builtin_amdgcn_sched_group_barrier(0x008, 2, 0);
        __builtin_amdgcn_sched_group_barrier(0x100, 4, 0);
    }
    __builtin_amdgcn_sched_group_barrier(0x008, 8, 0);
}

__global__ void __launch_bounds__(768)
fused_kernel(const u16* __restrict__ x, const u16* __restrict__ wq, const u16* __restrict__ wk,
             const u16* __restrict__ wv, const u16* __restrict__ wo, const u16* __restrict__ bq,
             const u16* __restrict__ bk, const u16* __restrict__ bv, const u16* __restrict__ shk,
             const u16* __restrict__ ssmk, const u16* __restrict__ ssmd, u16* __restrict__ out,
             char* ws) {
    unsigned long long dg0, dg1;
    asm volatile("s_memrealtime %0" : "=s"(dg0) :: "memory");
    extern __shared__ __attribute__((aligned(16))) char smem[];
    const int lane = threadIdx.x & 63;
    const int w = __builtin_amdgcn_readfirstlane(threadIdx.x >> 6);
    const int blk = blockIdx.x;
    const int tg = (blk & 7) * 4 + (blk >> 6);
    const int hs = (blk >> 3) & 7;
    const int r = lane & 31, h = lane >> 5;
    const __amdgpu_buffer_rsrc_t rs = __builtin_amdgcn_make_buffer_rsrc(ws, 0, WS_END, 0x00020000);
    asm volatile("" ::"s"(x), "s"(wq), "s"(wk), "s"(wv), "s"(wo), "s"(bq), "s"(bk), "s"(bv), "s"(shk),
                 "s"(ssmk), "s"(ssmd), "s"(out), "s"(ws));
    DG_RT(dg1);

    const bool kvp = hs < 2;
    const int xt = hs & 1;
    if (w >= 8) {
        __builtin_amdgcn_s_setprio(3);
        const int wb4 = w - 8;
#pragma unroll
        for (int j = 0; j < 8; ++j) {
            const int row = 32 * xt + 4 * j + wb4;
            dma16(x + (size_t)(64 * tg + row) * DM + ((lane ^ (row & 15)) << 3), smem + L_X + row * 1024);
        }
        if (kvp) {
            const u16* src = (wb4 < 2 ? wk : wv) + (wb4 & 1) * 4096 + lane * 8;
#pragma unroll
            for (int j = 0; j < 8; ++j) dma16(src + j * 512, smem + L_WK + (wb4 * 8 + j) * 1024);
        }
    } else {
        if (kvp) barrier();
#pragma unroll
        for (int j = 0; j < 4; ++j) {
            const int row = 32 * (xt ^ 1) + 8 * j + w;
            dma16(x + (size_t)(64 * tg + row) * DM + ((lane ^ (row & 15)) << 3), smem + L_X + row * 1024);
        }
        const u16* src = wq + (size_t)(64 * w + (lane >> 3)) * DM + 64 * hs + (((lane & 7) ^ (((lane >> 4) & 1) << 2)) << 3);
#pragma unroll
        for (int j = 0; j < 8; ++j) dma16(src + (size_t)j * 8 * DM, smem + L_W + (8 * w + j) * 1024);
    }
    u64 tag;
    asm volatile("s_load_dwordx2 %0, %1, 0x0\n\ts_waitcnt lgkmcnt(0)" : "=s"(tag) : "s"(ws) : "memory");
    tag += 1;

    const int tq = (lane & 15) >> 2, tp = lane & 3;
    if (w < 8) {
        drain();
        barrier();
        const int i = w >> 2, hd = w & 3, kh = hd & 1;
        const char* xb = smem + L_X + (32 * i + r) * 1024 + kh * 512;
        const int chunk = 4 * (hd >> 1) + 2 * ((lane >> 4) & 1) + (tp >> 1);
        const char* wb = smem + L_W + kh * 32768 + (8 * h + tq) * 128 + ((chunk ^ ((tq >> 1) << 2)) << 4) + 8 * (tp & 1);
        u32x4 fa[16], fb[16];
#pragma unroll
        for (int s = 0; s < 16; ++s) {
            fa[s] = tr_frag(wb + s * 2048, 512);
            fb[s] = ld16(xb + (s >> 3) * 256 + (((2 * (s & 7) + h) ^ (r & 15)) << 4));
        }
        f32x16 acc = {0};
#pragma unroll
        for (int s = 0; s < 16; ++s) acc = mfma32(fa[s], fb[s], acc);
        proj_schedule();
        f32x4 keep[2];
        {
            f32x4* xp = reinterpret_cast<f32x4*>(smem + L_QX + (w ^ 1) * 2048) + lane;
#pragma unroll
            for (int g = 0; g < 2; ++g) {
                f32x4 lo = {acc[4 * g], acc[4 * g + 1], acc[4 * g + 2], acc[4 * g + 3]};
                f32x4 hi = {acc[8 + 4 * g], acc[9 + 4 * g], acc[10 + 4 * g], acc[11 + 4 * g]};
                xp[g * 64] = kh ? lo : hi;
                keep[g] = kh ? hi : lo;
            }
        }
        barrier();
        dma16_dev(ws + WS_KV + w * 2048 + lane * 16, smem + L_K + w * 1024);
        dma16_dev(ws + WS_KV + w * 2048 + 1024 + lane * 16, smem + L_V + w * 1024);
        rendezvous8((unsigned)(size_t)(LDSP char*)smem + L_CNT + 12);
#pragma unroll
        for (int j = 1; j < 4; ++j) {
            dma16_dev(ws + WS_KV + (w + 8 * j) * 2048 + lane * 16, smem + L_K + (w + 8 * j) * 1024);
            dma16_dev(ws + WS_KV + (w + 8 * j) * 2048 + 1024 + lane * 16, smem + L_V + (w + 8 * j) * 1024);
        }
        const unsigned base = (unsigned)(size_t)(LDSP char*)smem;
        u32x4 q;
        {
            f32x4 a, b;
            asm volatile("ds_read_b128 %0, %2\n\tds_read_b128 %1, %2 offset:1024\n\ts_waitcnt lgkmcnt(0)"
                         : "=&v"(a), "=&v"(b) : "v"(base + L_QX + w * 2048 + lane * 16) : "memory");
            a = (a + keep[0]) * QSCALE;
            b = (b + keep[1]) * QSCALE;
            q = (u32x4){pack2(a[0], a[1]), pack2(a[2], a[3]), pack2(b[0], b[1]), pack2(b[2], b[3])};
        }
        const unsigned ka = base + L_K + lane * 16;
        const unsigned va = base + (r < 16 ? L_V + h * 256 + r * 16
                                           : L_CONST + (r == 16 ? 0 : ((r >= 20 && r < 28) ? 64 : 16)));
        const unsigned vstep = r < 16 ? 4096 : 0;
        asm volatile("s_waitcnt vmcnt(6)" ::: "memory");
        rendezvous8(base + L_CNT);
        float m = 0.f;
        f32x16 o;
        float l;
#pragma unroll 1
        for (int given = 0; given < 2; ++given) {
            o = attn_pass(ka, va, vstep, base + L_CNT, q, m, given);
            l = __shfl(o[8], r);
            float mag = 0.f;
#pragma unroll
            for (int j = 0; j < 8; ++j) mag += fabsf(o[j]);
            if (!__any(!(l < 1.0e18f) || !(mag < 3.0e38f))) break;
            m = row_max(smem + L_K + lane * 16, q);
        }
        const float inv = 1.0f / l;
        u32x4 of;
#pragma unroll
        for (int j = 0; j < 4; ++j) of[j] = pack2(o[2 * j] * inv, o[2 * j + 1] * inv);
        __builtin_amdgcn_raw_buffer_store_b128(
            of, rs, WS_OF + ((2 * tg + i) * 32 + 4 * hs + hd) * 1024 + lane * 16, 0, 16);
        publish(ws, WS_OFL + ((tg * 8 + hs) * 8 + w) * FL, tag, lane);
        return;
    }

    const int wb4 = w - 8;
    drain();
    if (kvp) barrier();
    if (wb4 < 2) {
        const int tt = 2 * tg + hs;
        if (kvp) {
            const int c = lane & 15, g = lane >> 4;
            const char* xb = smem + L_X + (32 * hs + c) * 1024;
            const char* wp = smem + (wb4 == 0 ? L_WK : L_WV) + (8 * g + tq) * 32 + 8 * tp;
            f32x4 a0 = {0}, a1 = {0};
            if (wb4 == 0) kv_proj<true>(xb, wp, g, c, a0, a1);
            else kv_proj<false>(xb, wp, g, c, a0, a1);
            const int off = WS_KV + tt * 2048 + wb4 * 1024 + (g & 1) * (wb4 ? 256 : 512) + c * 16 + (g >> 1) * 8;
            __builtin_amdgcn_raw_buffer_store_b64((u32x2){pack2(a0[0], a0[1]), pack2(a0[2], a0[3])}, rs, off, 0, 16);
            __builtin_amdgcn_raw_buffer_store_b64((u32x2){pack2(a1[0], a1[1]), pack2(a1[2], a1[3])}, rs,
                                                  off + (wb4 ? 512 : 256), 0, 16);
            drain();
            if (lane < 8)
                __hip_atomic_store((GLBP u64*)(ws + WS_KVF + lane * KVF_REP + (tt * 2 + wb4) * FL), tag,
                                   __ATOMIC_RELAXED, __HIP_MEMORY_SCOPE_AGENT);
        }
        barrier();
        if (wb4 == 0) {
            if (lane < 27) {
                const int b = lane / 3, p = lane - 3 * b;
                const unsigned v = p == 0 ? 0x3f803f80u : 0u;
                *reinterpret_cast<u32x4*>(smem + L_CONST + b * 512 + (p == 2 ? 64 : 16 * p)) = (u32x4){v, v, v, v};
            } else if (lane < 31) {
                *reinterpret_cast<u32x4*>(smem + L_CNT + (lane - 27) * 16) = (u32x4){0, 0, 0, 0};
            }
        }
        wait_flags<2>(ws, WS_KVF + (blk & 7) * KVF_REP + (wb4 * 64 + lane) * FL, true, tag);
        barrier();
        {
            lds_wait((LDSP unsigned*)(smem + L_CNT), 8);
            const char* src = ws + WS_KV + 32 * 2048 + wb4 * 1024 + lane * 16;
            char* dst = smem + (wb4 ? L_V : L_K) + 32 * 1024;
#pragma unroll 8
            for (int t = 0; t < 32; ++t) dma16_dev(src + t * 2048, dst + t * 1024);
            drain();
            lds_arrive((LDSP unsigned*)(smem + L_CNT + 4), lane);
        }
    } else {
        barrier();
        const int s0 = 64 * tg + 8 * ((lane >> 4) + 4 * (wb4 & 1));
        const int d0 = 64 * hs + 4 * (lane & 15);
        const __amdgpu_buffer_rsrc_t kr_ = __builtin_amdgcn_make_buffer_rsrc((void*)bk, 0, SEQ * DM * 2, 0x00020000);
        const __amdgpu_buffer_rsrc_t vr_ = __builtin_amdgcn_make_buffer_rsrc((void*)bv, 0, SEQ * DM * 2, 0x00020000);
        const int o0 = (s0 * DM + d0) * 2;
        int om = o0 - 3072;
        asm volatile("" : "+v"(om));
        u32x2 kr[11], vr[8];
#pragma unroll
        for (int j = 0; j < 3; ++j) kr[j] = __builtin_amdgcn_raw_buffer_load_b64(kr_, om + 1024 * j, 0, 0);
#pragma unroll
        for (int j = 0; j < 8; ++j) {
            kr[3 + j] = __builtin_amdgcn_raw_buffer_load_b64(kr_, o0 + 1024 * (j & 3), 4096 * (j >> 2), 0);
            vr[j] = __builtin_amdgcn_raw_buffer_load_b64(vr_, o0 + 1024 * (j & 3), 4096 * (j >> 2), 0);
        }
        const u32x4 sk0 = ld16(shk + d0 * 4), sk1 = ld16(shk + d0 * 4 + 8);
#pragma unroll
        for (int e = 0; e < 4; ++e) {
            const unsigned w01 = (e < 2 ? sk0 : sk1)[(e & 1) * 2], w23 = (e < 2 ? sk0 : sk1)[(e & 1) * 2 + 1];
            const float w0 = bflo(w01), w1 = bfhi(w01), w2 = bflo(w23), w3 = bfhi(w23);
            float kk[11], u[8];
#pragma unroll
            for (int j = 0; j < 11; ++j) kk[j] = (e & 1) ? bfhi(kr[j][e >> 1]) : bflo(kr[j][e >> 1]);
#pragma unroll
            for (int j = 0; j < 8; ++j) {
                const float a = fmaf(kk[j + 3], w0, fmaf(kk[j + 2], w1, fmaf(kk[j + 1], w2, kk[j] * w3)));
                u[j] = a * ((e & 1) ? bfhi(vr[j][e >> 1]) : bflo(vr[j][e >> 1]));
            }
            const u32x4 o = {pack2(u[7], u[6]), pack2(u[5], u[4]), pack2(u[3], u[2]), pack2(u[1], u[0])};
            __builtin_amdgcn_raw_buffer_store_b128(o, rs, WS_UR + ((d0 + e) * SEQ + (SEQ - 8) - s0) * 2, 0, 16);
        }
        publish(ws, WS_URF + ((hs * 32 + tg) * 2 + (wb4 & 1)) * FL, tag, lane);
        barrier();
    }

    {
        const int ch = wb4 >> 1, sh = wb4 & 1;
        const int d = 2 * blk + ch;
        const float dd = bf2f(ssmd[d]);
        wait_flags<24>(ws, WS_URF + ((d >> 6) * 64 + lane) * FL, true, tag);
        char* ul = smem + L_U + ch * 2 * U_RS;
        u32x4 uv[4];
#pragma unroll
        for (int j = 0; j < 4; ++j)
            uv[j] = __builtin_amdgcn_raw_buffer_load_b128(rs, WS_UR + d * 4096 + j * 1024 + lane * 16 + 2 * sh, 0, 16);
        if (sh && lane == 63) uv[3][3] &= 0xffffu;
        lds_wait((LDSP unsigned*)(smem + L_CNT), 8);
#pragma unroll
        for (int j = 0; j < 4; ++j)
            *reinterpret_cast<u32x4*>(ul + sh * U_RS + j * 1024 + lane * 16) = uv[j];
        if (lane < 2) *reinterpret_cast<u32x4*>(ul + sh * U_RS + 4096 + lane * 16) = (u32x4){0, 0, 0, 0};
        LDSP unsigned* pc = (LDSP unsigned*)(smem + L_CNT + 16 + 4 * ch);
        lds_arrive(pc, lane);
        lds_wait(pc, 2);
        const __amdgpu_buffer_rsrc_t hk = __builtin_amdgcn_make_buffer_rsrc((void*)(ssmk + (size_t)d * SEQ), 0, SEQ * 2, 0x00020000);
        if (sh) conv_wave<1, 2, 4, 7>(hk, ul, rs, WS_ZT + d * 4096, dd, lane);
        else conv_wave<0, 3, 5, 6>(hk, ul, rs, WS_ZT + d * 4096, dd, lane);
        publish(ws, WS_ZF + (d * 2 + sh) * FL, tag, lane);
    }

    if (wb4 < 2) {
        const int c8 = lane & 7;
        const int s0 = 64 * tg + 4 * ((lane >> 3) + 8 * wb4);
        const int d0 = 64 * hs + 8 * c8;
        u32x4 qr[4];
#pragma unroll
        for (int j = 0; j < 4; ++j) qr[j] = ld16(bq + (size_t)(s0 + j) * DM + d0);
        const int sh = (0x96 >> (tg >> 2)) & 1;
        wait_flags<24>(ws, WS_ZF + ((64 * hs + lane) * 2 + sh) * FL, true, tag);
        u32x2 z[8];
#pragma unroll
        for (int e = 0; e < 8; ++e)
            z[e] = __builtin_amdgcn_raw_buffer_load_b64(rs, WS_ZT + ((d0 + e) * SEQ + s0) * 2, 0, 16);
#pragma unroll
        for (int i = 0; i < 4; ++i) {
            u32x4 o;
#pragma unroll
            for (int j = 0; j < 4; ++j) {
                const unsigned za = z[2 * j][i >> 1], zb = z[2 * j + 1][i >> 1];
                const float a = (i & 1) ? bfhi(za) : bflo(za);
                const float b = (i & 1) ? bfhi(zb) : bflo(zb);
                o[j] = pack2(bflo(qr[i][j]) * a, bfhi(qr[i][j]) * b);
            }
            *reinterpret_cast<u32x4*>(out + (size_t)(s0 + i) * 1024 + 512 + d0) = o;
        }
    } else if (blk == 0 && wb4 == 2) {
#pragma unroll 1
        for (int j = 0; j < 8; ++j) wait_flags<24>(ws, WS_URF + (j * 64 + lane) * FL, true, tag);
        if (lane == 0) __hip_atomic_store((GLBP u64*)(ws + WS_EPOCH), tag, __ATOMIC_RELAXED, __HIP_MEMORY_SCOPE_AGENT);
    }

    u32x4 fe[8], fo[8];
    {
        const __amdgpu_buffer_rsrc_t wr = __builtin_amdgcn_make_buffer_rsrc((void*)wo, 0, DM * DM * 2, 0x00020000);
        const int vo = (4 * h * DM + 2 * (r ^ (8 * wb4))) * 2;
        const int so = (128 * wb4 * DM + 64 * hs) * 2;
        unsigned wd[8][8];
#pragma unroll
        for (int s = 0; s < 8; ++s)
#pragma unroll
            for (int j = 0; j < 8; ++j)
                wd[s][j] = __builtin_amdgcn_raw_buffer_load_b32(wr, vo + (j & 3) * 1024, so + (16 * s + 8 * (j >> 2)) * 1024, 0);
        __builtin_amdgcn_sched_barrier(0);
#pragma unroll
        for (int s = 0; s < 8; ++s) {
#pragma unroll
            for (int j = 0; j < 4; ++j) {
                fe[s][j] = __builtin_amdgcn_perm(wd[s][2 * j + 1], wd[s][2 * j], 0x05040100u);
                fo[s][j] = __builtin_amdgcn_perm(wd[s][2 * j + 1], wd[s][2 * j], 0x07060302u);
            }
            asm volatile("" : "+v"(fe[s]), "+v"(fo[s]));
        }
    }
    LDSP unsigned* cnt = (LDSP unsigned*)(smem + L_CNT + 32);
    lds_arrive(cnt, lane);
    while (__hip_atomic_load((LDSP unsigned*)(smem + L_CNT + 8), __ATOMIC_RELAXED, __HIP_MEMORY_SCOPE_WORKGROUP) < 4u)
        __builtin_amdgcn_s_sleep(8);
#pragma unroll 1
    for (int i = 0; i < 2; ++i) {
        wait_flags<1>(ws, WS_OFL + ((tg * 8 + 2 * wb4 + (lane >> 2)) * 8 + 4 * i + (lane & 3)) * FL, lane < 8, tag);
        u32x4 of[8];
#pragma unroll
        for (int s = 0; s < 8; ++s)
            of[s] = __builtin_amdgcn_raw_buffer_load_b128(
                rs, lane * 16 + (s & 3) * 1024, WS_OF + ((2 * tg + i) * 32 + 8 * wb4 + (s & 4)) * 1024, 16);
        f32x16 ae = {0}, ao = {0};
#pragma unroll
        for (int s = 0; s < 8; ++s) {
            ae = mfma32(fe[s], of[s], ae);
            ao = mfma32(fo[s], of[s], ao);
        }
        lds_wait(cnt, 4 + 8 * i);
#pragma unroll
        for (int g = 1; g < 4; ++g) {
            f32x4* p = reinterpret_cast<f32x4*>(smem + L_RED + ((g ^ wb4) * 3 + g - 1) * 2048) + lane;
            p[0] = (f32x4){ae[4 * g], ae[4 * g + 1], ae[4 * g + 2], ae[4 * g + 3]};
            p[64] = (f32x4){ao[4 * g], ao[4 * g + 1], ao[4 * g + 2], ao[4 * g + 3]};
        }
        lds_arrive(cnt, lane);
        lds_wait(cnt, 8 + 8 * i);
        f32x4 e = {ae[0], ae[1], ae[2], ae[3]}, o = {ao[0], ao[1], ao[2], ao[3]};
        const f32x4* p = reinterpret_cast<const f32x4*>(smem + L_RED + wb4 * 3 * 2048) + lane;
#pragma unroll
        for (int g = 0; g < 3; ++g) {
            e += p[g * 128];
            o += p[g * 128 + 64];
        }
        const u32x4 res = {pack2(e[0], o[0]), pack2(e[1], o[1]), pack2(e[2], o[2]), pack2(e[3], o[3])};
        *reinterpret_cast<u32x4*>(out + (size_t)(64 * tg + 32 * i + r) * 1024 + 64 * hs + 16 * wb4 + 8 * h) = res;
        lds_arrive(cnt, lane);
    }
    { unsigned long long e, n; DG_RT(e); if (blockIdx.x == 100 && w == 8) { do { DG_RT(n); } while (n - e < 4 * (dg1 - dg0)); } }
}

extern "C" void kernel_launch(void* const* d_in, const int* in_sizes, int n_in, void* d_out,
                              int out_size, void* d_ws, size_t ws_size, hipStream_t stream) {
    (void)hipFuncSetAttribute((const void*)fused_kernel, hipFuncAttributeMaxDynamicSharedMemorySize, LDS_BYTES);
    fused_kernel<<<256, 768, LDS_BYTES, stream>>>(
        (const u16*)d_in[0], (const u16*)d_in[1], (const u16*)d_in[2], (const u16*)d_in[3],
        (const u16*)d_in[4], (const u16*)d_in[5], (const u16*)d_in[6], (const u16*)d_in[7],
        (const u16*)d_in[8], (const u16*)d_in[9], (const u16*)d_in[10], (u16*)d_out, (char*)d_ws);
}
```

```cpp
#include <hip/hip_runtime.h>
#include <hip/hip_bf16.h>
#define CSP 3

typedef __attribute__((ext_vector_type(8))) __bf16 bf16x8;
typedef __attribute__((ext_vector_type(2))) __bf16 bf16x2;
typedef __attribute__((ext_vector_type(16))) float f32x16;
typedef __attribute__((ext_vector_type(4))) float f32x4;
typedef __attribute__((ext_vector_type(2))) float f32x2;
typedef __attribute__((ext_vector_type(4))) unsigned u32x4;
typedef __attribute__((ext_vector_type(2))) unsigned u32x2;
typedef __attribute__((ext_vector_type(4))) short s16x4;
typedef unsigned long long u64;
typedef unsigned short u16;
#define LDSP __attribute__((address_space(3)))
#define GLBP __attribute__((address_space(1)))

constexpr int SEQ = 2048;
constexpr int DM = 512;

constexpr int FL = 128;
constexpr int WS_EPOCH = 0;
constexpr int KVF_REP = 128 * FL + 4096;
constexpr int WS_KVF = 4096;
constexpr int WS_URF = WS_KVF + 8 * KVF_REP;
constexpr int WS_ZF = WS_URF + 512 * FL;
constexpr int WS_OFL = WS_ZF + 1024 * FL;
constexpr int WS_KV = WS_OFL + 2048 * FL;
constexpr int WS_UR = WS_KV + 131072;
constexpr int WS_ZT = WS_UR + 2097152 + 4096;
constexpr int WS_OF = WS_ZT + 2097152;
constexpr int WS_END = WS_OF + 2097152;

constexpr int L_X = 0;
constexpr int L_W = 65536;
constexpr int L_WK = 131072;
constexpr int L_WV = 147456;
constexpr int L_K = 0;
constexpr int L_V = 65536;
constexpr int L_CONST = 131072;
constexpr int L_CNT = L_CONST + 9 * 512;
constexpr int L_U = L_CNT + 64;
constexpr int U_RS = 4160;
constexpr int L_RED = L_U;
constexpr int L_QX = 147456;
constexpr int LDS_BYTES = 163840;

constexpr float QSCALE = 0.25f * 1.4426950408889634f;
constexpr unsigned SPIN_CAP = 1u << 18;

__device__ __forceinline__ float bf2f(u16 v) { return __uint_as_float(((unsigned)v) << 16); }
__device__ __forceinline__ float bflo(unsigned v) { return __uint_as_float(v << 16); }
__device__ __forceinline__ float bfhi(unsigned v) { return __uint_as_float(v & 0xffff0000u); }
__device__ __forceinline__ unsigned pack2(float lo, float hi) {
    f32x2 f = {lo, hi};
    bf16x2 b = __builtin_convertvector(f, bf16x2);
    return __builtin_bit_cast(unsigned, b);
}
__device__ __forceinline__ float rbf(float x) { return (float)(__bf16)x; }
__device__ __forceinline__ bf16x8 as_bf(u32x4 v) { return __builtin_bit_cast(bf16x8, v); }
__device__ __forceinline__ f32x16 mfma32(u32x4 a, u32x4 b, f32x16 c) {
    return __builtin_amdgcn_mfma_f32_32x32x16_bf16(as_bf(a), as_bf(b), c, 0, 0, 0);
}
__device__ __forceinline__ f32x4 mfma16(u32x4 a, u32x4 b, f32x4 c) {
    return __builtin_amdgcn_mfma_f32_16x16x32_bf16(as_bf(a), as_bf(b), c, 0, 0, 0);
}
__device__ __forceinline__ u32x4 ld16(const void* p) { return *reinterpret_cast<const u32x4*>(p); }
__device__ __forceinline__ u32x2 ld8(const void* p) { return *reinterpret_cast<const u32x2*>(p); }
__device__ __forceinline__ u32x4 tr_frag(const char* p, int second) {
    const s16x4 a = __builtin_amdgcn_ds_read_tr16_b64_v4i16((LDSP s16x4*)p);
    const s16x4 b = __builtin_amdgcn_ds_read_tr16_b64_v4i16((LDSP s16x4*)(p + second));
    const u32x2 a2 = __builtin_bit_cast(u32x2, a), b2 = __builtin_bit_cast(u32x2, b);
    return (u32x4){a2[0], a2[1], b2[0], b2[1]};
}
__device__ __forceinline__ void dma16(const void* src, char* dst) {
    __builtin_amdgcn_global_load_lds(src, (LDSP void*)dst, 16, 0, 0);
}
__device__ __forceinline__ void dma16_dev(const void* src, char* dst) {
    __builtin_amdgcn_global_load_lds(src, (LDSP void*)dst, 16, 0, 16);
}
__device__ __forceinline__ void barrier() {
    asm volatile("s_waitcnt lgkmcnt(0)\n\ts_barrier" ::: "memory");
}
__device__ __forceinline__ void proj_schedule() {
    __builtin_amdgcn_sched_group_barrier(0x100, 12, 0);
#pragma unroll
    for (int s = 0; s < 12; ++s) {
        __builtin_amdgcn_sched_group_barrier(0x008, 1, 0);
        __builtin_amdgcn_sched_group_barrier(0x100, 3, 0);
    }
    __builtin_amdgcn_sched_group_barrier(0x008, 4, 0);
}
__device__ __forceinline__ void drain() { asm volatile("s_waitcnt vmcnt(0)" ::: "memory"); }

__device__ __forceinline__ void lds_arrive(LDSP unsigned* c, int lane) {
    asm volatile("s_waitcnt lgkmcnt(0)" ::: "memory");
    if (lane == 0) __hip_atomic_fetch_add(c, 1u, __ATOMIC_RELAXED, __HIP_MEMORY_SCOPE_WORKGROUP);
}
__device__ __forceinline__ void lds_wait(LDSP unsigned* c, unsigned n) {
    while (__hip_atomic_load(c, __ATOMIC_RELAXED, __HIP_MEMORY_SCOPE_WORKGROUP) < n) {}
    asm volatile("" ::: "memory");
}

__device__ __forceinline__ void publish(char* ws, int off, u64 tag, int lane) {
    drain();
    if (lane == 0) __hip_atomic_store((GLBP u64*)(ws + off), tag, __ATOMIC_RELAXED, __HIP_MEMORY_SCOPE_AGENT);
}
template <int SLEEP>
__device__ __forceinline__ void wait_flags(char* ws, int off, bool active, u64 tag) {
#pragma unroll 1
    for (unsigned spin = 0; spin < SPIN_CAP; ++spin) {
        u64 v = tag;
        if (active) v = __hip_atomic_load((GLBP u64*)(ws + off), __ATOMIC_RELAXED, __HIP_MEMORY_SCOPE_AGENT);
        if (__all(v == tag)) break;
        __builtin_amdgcn_s_sleep(SLEEP);
    }
    asm volatile("" ::: "memory");
}

#define STR2(x) #x
#define STR(x) STR2(x)
#define EX(r) "v_exp_f32 v" STR(r) ", v" STR(r) "\n"
#define CV(d, a, b) "v_cvt_pk_bf16_f32 v" STR(d) ", v" STR(a) ", v" STR(b) "\n"
#define TILE(x0, x1, x2, x3, x4, x5, x6, x7, x8, x9, x10, x11, x12, x13, x14, x15, NEXT, u)     \
    "s_waitcnt lgkmcnt(2)\n"                                                                    \
    "v_mfma_f32_32x32x16_bf16 " NEXT ", v[104:107], %[q], v[116:131]\n"                         \
    "ds_read_b128 v[104:107], %[ka] offset:" STR((u + 2) * 1024) "\n"                           \
    EX(x0) EX(x1) EX(x2) EX(x3) CV(96, x0, x1) EX(x4) EX(x5) CV(97, x2, x3)                     \
    "s_waitcnt lgkmcnt(2)\n"                                                                    \
    "v_mfma_f32_32x32x16_bf16 %[o], v[112:115], v[100:103], %[o]\n"                             \
    "ds_read_b128 v[112:115], %[va] offset:" STR((2 * u + 1) * 512) "\n"                        \
    EX(x6) EX(x7) CV(98, x4, x5) EX(x8) CV(99, x6, x7) EX(x9) EX(x10) EX(x11)                   \
    "s_waitcnt lgkmcnt(2)\n"                                                                    \
    "v_mfma_f32_32x32x16_bf16 %[o], v[108:111], v[96:99], %[o]\n"                               \
    "ds_read_b128 v[108:111], %[va] offset:" STR((2 * u + 2) * 512) "\n"                        \
    CV(100, x8, x9) EX(x12) EX(x13) CV(101, x10, x11) EX(x14) EX(x15) CV(102, x12, x13)         \
    "s_nop 0\n" CV(103, x14, x15)
#define TILE_X(u) TILE(64, 65, 66, 67, 68, 69, 70, 71, 72, 73, 74, 75, 76, 77, 78, 79, "v[80:95]", u)
#define TILE_Y(u) TILE(80, 81, 82, 83, 84, 85, 86, 87, 88, 89, 90, 91, 92, 93, 94, 95, "v[64:79]", u)
#define MOVC(r) "v_mov_b32 v" STR(r) ", v116\n"
#define LDS_WAIT(off, n, lbl)                                                                   \
    lbl ":\n"                                                                                   \
    "ds_read_b32 v96, %[ca] offset:" STR(off) "\n"                                              \
    "s_waitcnt lgkmcnt(0)\n"                                                                    \
    "v_readfirstlane_b32 %[st], v96\n"                                                          \
    "s_cmp_lt_u32 %[st], " STR(n) "\n"                                                          \
    "s_cbranch_scc1 " lbl "b\n"
#define LDS_ARRIVE(off)                                                                         \
    "s_mov_b64 %[sx], exec\n"                                                                   \
    "s_mov_b64 exec, 1\n"                                                                       \
    "v_mov_b32 v96, 1\n"                                                                        \
    "ds_add_u32 %[ca], v96 offset:" STR(off) "\n"                                               \
    "s_mov_b64 exec, %[sx]\n"
__device__ __forceinline__ void rendezvous8(unsigned ca) {
    int st;
    u64 sx;
    asm volatile(LDS_ARRIVE(0) LDS_WAIT(0, 8, "20")
                 : [st] "=&s"(st), [sx] "=&s"(sx) : [ca] "v"(ca) : "v96", "scc", "memory");
}
#define ATT_CLOB                                                                                \
    "v64", "v65", "v66", "v67", "v68", "v69", "v70", "v71", "v72", "v73", "v74", "v75", "v76",  \
    "v77", "v78", "v79", "v80", "v81", "v82", "v83", "v84", "v85", "v86", "v87", "v88", "v89",  \
    "v90", "v91", "v92", "v93", "v94", "v95", "v96", "v97", "v98", "v99", "v100", "v101",       \
    "v102", "v103", "v104", "v105", "v106", "v107", "v108", "v109", "v110", "v111", "v112",     \
    "v113", "v114", "v115", "v116", "v117", "v118", "v119", "v120", "v121", "v122", "v123",     \
    "v124", "v125", "v126", "v127", "v128", "v129", "v130", "v131"

#define ITER_STAMP
#define ITER_OUT
__device__ __forceinline__ f32x16 attn_pass(unsigned ka, unsigned va, unsigned vstep, unsigned ca,
                                            u32x4 q, float mgiven, int given) {
    f32x16 o = {0};
    int cnt, st;
    u64 sx;
    asm volatile(
        "ds_read_b128 v[104:107], %[ka]\n"
        "v_mov_b32 v116, %[mg]\n"
        "s_cmp_lg_u32 %[gv], 0\n"
        "s_waitcnt lgkmcnt(0)\n"
        "s_cbranch_scc1 11f\n"
        "v_mfma_f32_32x32x16_bf16 v[64:79], v[104:107], %[q], 0\n"
        "s_nop 11\n"
        "v_max3_f32 v96, v64, v65, v66\n"
        "v_max3_f32 v97, v67, v68, v69\n"
        "v_max3_f32 v98, v70, v71, v72\n"
        "v_max3_f32 v99, v73, v74, v75\n"
        "v_max3_f32 v96, v96, v76, v77\n"
        "v_max3_f32 v97, v97, v78, v79\n"
        "v_max3_f32 v96, v96, v98, v99\n"
        "v_max_f32 v96, v96, v97\n"
        "v_mov_b32 v97, v96\n"
        "s_nop 1\n"
        "v_permlane32_swap_b32 v96, v97\n"
        "s_nop 1\n"
        "v_max_f32 v116, v96, v97\n"
        "11:\n"
        "v_xor_b32 v116, 0x80000000, v116\n"
        MOVC(117) MOVC(118) MOVC(119) MOVC(120) MOVC(121) MOVC(122) MOVC(123) MOVC(124)
        MOVC(125) MOVC(126) MOVC(127) MOVC(128) MOVC(129) MOVC(130) MOVC(131)
        "v_mov_b32 v100, 0\n v_mov_b32 v101, 0\n v_mov_b32 v102, 0\n v_mov_b32 v103, 0\n"
        "s_movk_i32 %[cnt], 16\n"
        "s_setprio 2\n"
        "v_mfma_f32_32x32x16_bf16 v[64:79], v[104:107], %[q], v[116:131]\n"
        "ds_read_b128 v[104:107], %[ka] offset:1024\n"
        "ds_read_b128 v[112:115], %[va]\n"
        "ds_read_b128 v[108:111], %[va]\n"
        "s_nop 10\n"
        "12:\n"
        TILE_X(0) TILE_Y(1) TILE_X(2) TILE_Y(3)
        "v_add_u32 %[ka], 4096, %[ka]\n"
        "v_add_u32 %[va], %[vs], %[va]\n"
        "s_cmp_lg_u32 %[cnt], 16\n"
        "s_cbranch_scc1 14f\n"
        "s_waitcnt vmcnt(0)\n"
        LDS_ARRIVE(24)
        LDS_WAIT(24, 8, "13")
        "14:\n"
        "s_cmp_lg_u32 %[cnt], 10\n"
        "s_cbranch_scc1 15f\n"
        LDS_WAIT(4, 2, "17")
        "s_setprio 1\n"
        "15:\n"
        "s_cmp_lg_u32 %[cnt], 3\n"
        "s_cbranch_scc1 16f\n"
        "s_setprio 0\n"
        LDS_ARRIVE(8)
        "s_waitcnt lgkmcnt(0)\n"
        "16:\n"
        ITER_STAMP
        "s_sub_u32 %[cnt], %[cnt], 1\n"
        "s_cmp_lg_u32 %[cnt], 0\n"
        "s_cbranch_scc1 12b\n"
        "s_waitcnt lgkmcnt(0)\n"
        "v_mfma_f32_32x32x16_bf16 %[o], v[112:115], v[100:103], %[o]\n"
        "s_nop 15\n"
        : [o] "+v"(o), [ka] "+v"(ka), [va] "+v"(va), [cnt] "=&s"(cnt), [st] "=&s"(st), [sx] "=&s"(sx) ITER_OUT
        : [q] "v"(q), [vs] "v"(vstep), [ca] "v"(ca), [mg] "v"(mgiven), [gv] "s"(given)
        : ATT_CLOB, "scc", "memory", "s90", "s91", "s92", "s93");
    return o;
}

__device__ __forceinline__ float row_max(const char* kb, u32x4 q) {
    float m = -__builtin_inff();
    const f32x16 z = {0};
    for (int t = 0; t < 64; ++t) {
        const f32x16 s = mfma32(ld16(kb + t * 1024), q, z);
#pragma unroll
        for (int j = 0; j < 16; ++j) m = fmaxf(m, s[j]);
    }
    return fmaxf(m, __shfl_xor(m, 32));
}

#define GAP __builtin_amdgcn_sched_barrier(0); asm volatile("s_sleep 1"); __builtin_amdgcn_sched_barrier(0);
template <int T0, int T1, int T2, int T3>
__device__ __forceinline__ void conv_wave(__amdgpu_buffer_rsrc_t hr, const char* ul,
                                          __amdgpu_buffer_rsrc_t rs, int zoff, float dd, int lane) {
    constexpr int NF = T3 + 1;
    const int r = lane & 15, g = lane >> 4;
    const int ns = (SEQ - 1) - 16 * (g >> 1) - r + 8 * (g & 1);
    unsigned ap = (unsigned)(size_t)(LDSP const char*)ul + ((ns & 1) ? (U_RS + 2 * (ns - 1)) : 2 * ns) - 512 * (NF - 1);
    int b0 = 2 * (16 * r - 16 * (g >> 1) + 8 * (g & 1));
    f32x4 c0 = {0}, c1 = {0}, c2 = {0}, c3 = {0};
#pragma unroll 1
    for (int rho = 0; rho < 8; ++rho) {
        u32x4 A[NF], B[NF];
        int b1 = b0 + 512;
        asm volatile("" : "+v"(b1), "+v"(ap));
        B[0] = __builtin_amdgcn_raw_buffer_load_b128(hr, b0, 0, 0);
#pragma unroll
        for (int b = 1; b < NF; ++b) B[b] = __builtin_amdgcn_raw_buffer_load_b128(hr, b1 + 512 * (b - 1), 0, 0);
#pragma unroll
        for (int a = 0; a < NF; ++a) {
            volatile LDSP const unsigned* p = (volatile LDSP const unsigned*)(size_t)(ap + 512 * (NF - 1 - a));
            A[a] = (u32x4){p[0], p[1], p[2], p[3]};
        }
        __builtin_amdgcn_sched_barrier(0);
#pragma unroll
        for (int a = 0; a < NF; ++a) {
            c3 = mfma16(A[a], B[T3 - a], c3);
            GAP
            if (a <= T2) { c2 = mfma16(A[a], B[a <= T2 ? T2 - a : 0], c2); GAP }
            if (a <= T1) { c1 = mfma16(A[a], B[a <= T1 ? T1 - a : 0], c1); GAP }
            if (a <= T0) { c0 = mfma16(A[a], B[a <= T0 ? T0 - a : 0], c0); GAP }
        }
        b0 -= 64;
        ap -= 64;
    }
    const int ts[4] = {T0, T1, T2, T3};
    const f32x4 cs[4] = {c0, c1, c2, c3};
#pragma unroll
    for (int i = 0; i < 4; ++i) {
        const int s = 256 * ts[i] + 16 * r + 4 * g;
        const u32x2 uu = ld8(ul + 2 * ((SEQ - 4) - s));
        const u32x2 o = {pack2(fmaf(bfhi(uu[1]), dd, cs[i][0]), fmaf(bflo(uu[1]), dd, cs[i][1])),
                         pack2(fmaf(bfhi(uu[0]), dd, cs[i][2]), fmaf(bflo(uu[0]), dd, cs[i][3]))};
        __builtin_amdgcn_raw_buffer_store_b64(o, rs, zoff + 2 * s, 0, 16);
    }
}

template <bool WFIRST>
__device__ __forceinline__ void kv_proj(const char* xb, const char* wp, int g, int c, f32x4& a0, f32x4& a1) {
#pragma unroll
    for (int s = 0; s < 16; ++s) {
        const int xo = (s >> 2) * 256 + (((4 * (s & 3) + g) ^ c) << 4);
        const u32x4 fw = tr_frag(wp + s * 1024, 128), f0 = ld16(xb + xo), f1 = ld16(xb + xo + 16384);
        a0 = WFIRST ? mfma16(fw, f0, a0) : mfma16(f0, fw, a0);
        a1 = WFIRST ? mfma16(fw, f1, a1) : mfma16(f1, fw, a1);
    }
    __builtin_amdgcn_sched_group_barrier(0x100, 16, 0);
#pragma unroll
    for (int s = 0; s < 12; ++s) {
        __builtin_amdgcn_sched_group_barrier(0x008, 2, 0);
        __builtin_amdgcn_sched_group_barrier(0x100, 4, 0);
    }
    __builtin_amdgcn_sched_group_barrier(0x008, 8, 0);
}

__global__ void __launch_bounds__(768)
fused_kernel(const u16* __restrict__ x, const u16* __restrict__ wq, const u16* __restrict__ wk,
             const u16* __restrict__ wv, const u16* __restrict__ wo, const u16* __restrict__ bq,
             const u16* __restrict__ bk, const u16* __restrict__ bv, const u16* __restrict__ shk,
             const u16* __restrict__ ssmk, const u16* __restrict__ ssmd, u16* __restrict__ out,
             char* ws) {
    extern __shared__ __attribute__((aligned(16))) char smem[];
    const int lane = threadIdx.x & 63;
    const int w = __builtin_amdgcn_readfirstlane(threadIdx.x >> 6);
    const int blk = blockIdx.x;
    const int tg = (blk & 7) * 4 + (blk >> 6);
    const int hs = (blk >> 3) & 7;
    const int r = lane & 31, h = lane >> 5;
    const __amdgpu_buffer_rsrc_t rs = __builtin_amdgcn_make_buffer_rsrc(ws, 0, WS_END, 0x00020000);
    asm volatile("" ::"s"(x), "s"(wq), "s"(wk), "s"(wv), "s"(wo), "s"(bq), "s"(bk), "s"(bv), "s"(shk),
                 "s"(ssmk), "s"(ssmd), "s"(out), "s"(ws));

    const bool kvp = hs < 2;
    const int xt = hs & 1;
    if (w >= 8) {
        __builtin_amdgcn_s_setprio(3);
        const int wb4 = w - 8;
#pragma unroll
        for (int j = 0; j < 8; ++j) {
            const int row = 32 * xt + 4 * j + wb4;
            dma16(x + (size_t)(64 * tg + row) * DM + ((lane ^ (row & 15)) << 3), smem + L_X + row * 1024);
        }
        if (kvp) {
            const u16* src = (wb4 < 2 ? wk : wv) + (wb4 & 1) * 4096 + lane * 8;
#pragma unroll
            for (int j = 0; j < 8; ++j) dma16(src + j * 512, smem + L_WK + (wb4 * 8 + j) * 1024);
        }
    } else {
        if (kvp) barrier();
#pragma unroll
        for (int j = 0; j < 4; ++j) {
            const int row = 32 * (xt ^ 1) + 8 * j + w;
            dma16(x + (size_t)(64 * tg + row) * DM + ((lane ^ (row & 15)) << 3), smem + L_X + row * 1024);
        }
        const u16* src = wq + (size_t)(64 * w + (lane >> 3)) * DM + 64 * hs + (((lane & 7) ^ (((lane >> 4) & 1) << 2)) << 3);
#pragma unroll
        for (int j = 0; j < 8; ++j) dma16(src + (size_t)j * 8 * DM, smem + L_W + (8 * w + j) * 1024);
    }
    u64 tag;
    asm volatile("s_load_dwordx2 %0, %1, 0x0\n\ts_waitcnt lgkmcnt(0)" : "=s"(tag) : "s"(ws) : "memory");
    tag += 1;

    const int tq = (lane & 15) >> 2, tp = lane & 3;
    if (w < 8) {
        drain();
        barrier();
        const int i = w >> 2, hd = w & 3, kh = hd & 1;
        const char* xb = smem + L_X + (32 * i + r) * 1024 + kh * 512;
        const int chunk = 4 * (hd >> 1) + 2 * ((lane >> 4) & 1) + (tp >> 1);
        const char* wb = smem + L_W + kh * 32768 + (8 * h + tq) * 128 + ((chunk ^ ((tq >> 1) << 2)) << 4) + 8 * (tp & 1);
        u32x4 fa[16], fb[16];
#pragma unroll
        for (int s = 0; s < 16; ++s) {
            fa[s] = tr_frag(wb + s * 2048, 512);
            fb[s] = ld16(xb + (s >> 3) * 256 + (((2 * (s & 7) + h) ^ (r & 15)) << 4));
        }
        f32x16 acc = {0};
#pragma unroll
        for (int s = 0; s < 16; ++s) acc = mfma32(fa[s], fb[s], acc);
        proj_schedule();
        f32x4 keep[2];
        {
            f32x4* xp = reinterpret_cast<f32x4*>(smem + L_QX + (w ^ 1) * 2048) + lane;
#pragma unroll
            for (int g = 0; g < 2; ++g) {
                f32x4 lo = {acc[4 * g], acc[4 * g + 1], acc[4 * g + 2], acc[4 * g + 3]};
                f32x4 hi = {acc[8 + 4 * g], acc[9 + 4 * g], acc[10 + 4 * g], acc[11 + 4 * g]};
                xp[g * 64] = kh ? lo : hi;
                keep[g] = kh ? hi : lo;
            }
        }
        barrier();
        dma16_dev(ws + WS_KV + w * 2048 + lane * 16, smem + L_K + w * 1024);
        dma16_dev(ws + WS_KV + w * 2048 + 1024 + lane * 16, smem + L_V + w * 1024);
        rendezvous8((unsigned)(size_t)(LDSP char*)smem + L_CNT + 12);
#pragma unroll
        for (int j = 1; j < 4; ++j) {
            dma16_dev(ws + WS_KV + (w + 8 * j) * 2048 + lane * 16, smem + L_K + (w + 8 * j) * 1024);
            dma16_dev(ws + WS_KV + (w + 8 * j) * 2048 + 1024 + lane * 16, smem + L_V + (w + 8 * j) * 1024);
        }
        const unsigned base = (unsigned)(size_t)(LDSP char*)smem;
        u32x4 q;
        {
            f32x4 a, b;
            asm volatile("ds_read_b128 %0, %2\n\tds_read_b128 %1, %2 offset:1024\n\ts_waitcnt lgkmcnt(0)"
                         : "=&v"(a), "=&v"(b) : "v"(base + L_QX + w * 2048 + lane * 16) : "memory");
            a = (a + keep[0]) * QSCALE;
            b = (b + keep[1]) * QSCALE;
            q = (u32x4){pack2(a[0], a[1]), pack2(a[2], a[3]), pack2(b[0], b[1]), pack2(b[2], b[3])};
        }
        const unsigned ka = base + L_K + lane * 16;
        const unsigned va = base + (r < 16 ? L_V + h * 256 + r * 16
                                           : L_CONST + (r == 16 ? 0 : ((r >= 20 && r < 28) ? 64 : 16)));
        const unsigned vstep = r < 16 ? 4096 : 0;
        asm volatile("s_waitcnt vmcnt(6)" ::: "memory");
        rendezvous8(base + L_CNT);
        float m = 0.f;
        f32x16 o;
        float l;
#pragma unroll 1
        for (int given = 0; given < 2; ++given) {
            o = attn_pass(ka, va, vstep, base + L_CNT, q, m, given);
            l = __shfl(o[8], r);
            float mag = 0.f;
#pragma unroll
            for (int j = 0; j < 8; ++j) mag += fabsf(o[j]);
            if (!__any(!(l < 1.0e18f) || !(mag < 3.0e38f))) break;
            m = row_max(smem + L_K + lane * 16, q);
        }
        const float inv = 1.0f / l;
        u32x4 of;
#pragma unroll
        for (int j = 0; j < 4; ++j) of[j] = pack2(o[2 * j] * inv, o[2 * j + 1] * inv);
        __builtin_amdgcn_raw_buffer_store_b128(
            of, rs, WS_OF + ((2 * tg + i) * 32 + 4 * hs + hd) * 1024 + lane * 16, 0, 16);
        publish(ws, WS_OFL + ((tg * 8 + hs) * 8 + w) * FL, tag, lane);
        return;
    }

    const int wb4 = w - 8;
    drain();
    if (kvp) barrier();
    if (wb4 < 2) {
        const int tt = 2 * tg + hs;
        if (kvp) {
            const int c = lane & 15, g = lane >> 4;
            const char* xb = smem + L_X + (32 * hs + c) * 1024;
            const char* wp = smem + (wb4 == 0 ? L_WK : L_WV) + (8 * g + tq) * 32 + 8 * tp;
            f32x4 a0 = {0}, a1 = {0};
            if (wb4 == 0) kv_proj<true>(xb, wp, g, c, a0, a1);
            else kv_proj<false>(xb, wp, g, c, a0, a1);
            const int off = WS_KV + tt * 2048 + wb4 * 1024 + (g & 1) * (wb4 ? 256 : 512) + c * 16 + (g >> 1) * 8;
            __builtin_amdgcn_raw_buffer_store_b64((u32x2){pack2(a0[0], a0[1]), pack2(a0[2], a0[3])}, rs, off, 0, 16);
            __builtin_amdgcn_raw_buffer_store_b64((u32x2){pack2(a1[0], a1[1]), pack2(a1[2], a1[3])}, rs,
                                                  off + (wb4 ? 512 : 256), 0, 16);
            drain();
            if (lane < 8)
                __hip_atomic_store((GLBP u64*)(ws + WS_KVF + lane * KVF_REP + (tt * 2 + wb4) * FL), tag,
                                   __ATOMIC_RELAXED, __HIP_MEMORY_SCOPE_AGENT);
        }
        barrier();
        if (wb4 == 0) {
            if (lane < 27) {
                const int b = lane / 3, p = lane - 3 * b;
                const unsigned v = p == 0 ? 0x3f803f80u : 0u;
                *reinterpret_cast<u32x4*>(smem + L_CONST + b * 512 + (p == 2 ? 64 : 16 * p)) = (u32x4){v, v, v, v};
            } else if (lane < 31) {
                *reinterpret_cast<u32x4*>(smem + L_CNT + (lane - 27) * 16) = (u32x4){0, 0, 0, 0};
            }
        }
        wait_flags<2>(ws, WS_KVF + (blk & 7) * KVF_REP + (wb4 * 64 + lane) * FL, true, tag);
        barrier();
        {
            lds_wait((LDSP unsigned*)(smem + L_CNT), 8);
            const char* src = ws + WS_KV + 32 * 2048 + wb4 * 1024 + lane * 16;
            char* dst = smem + (wb4 ? L_V : L_K) + 32 * 1024;
#pragma unroll 8
            for (int t = 0; t < 32; ++t) dma16_dev(src + t * 2048, dst + t * 1024);
            drain();
            lds_arrive((LDSP unsigned*)(smem + L_CNT + 4), lane);
        }
    } else {
        barrier();
        const int s0 = 64 * tg + 8 * ((lane >> 4) + 4 * (wb4 & 1));
        const int d0 = 64 * hs + 4 * (lane & 15);
        const __amdgpu_buffer_rsrc_t kr_ = __builtin_amdgcn_make_buffer_rsrc((void*)bk, 0, SEQ * DM * 2, 0x00020000);
        const __amdgpu_buffer_rsrc_t vr_ = __builtin_amdgcn_make_buffer_rsrc((void*)bv, 0, SEQ * DM * 2, 0x00020000);
        const int o0 = (s0 * DM + d0) * 2;
        int om = o0 - 3072;
        asm volatile("" : "+v"(om));
        u32x2 kr[11], vr[8];
#pragma unroll
        for (int j = 0; j < 3; ++j) kr[j] = __builtin_amdgcn_raw_buffer_load_b64(kr_, om + 1024 * j, 0, 0);
#pragma unroll
        for (int j = 0; j < 8; ++j) {
            kr[3 + j] = __builtin_amdgcn_raw_buffer_load_b64(kr_, o0 + 1024 * (j & 3), 4096 * (j >> 2), 0);
            vr[j] = __builtin_amdgcn_raw_buffer_load_b64(vr_, o0 + 1024 * (j & 3), 4096 * (j >> 2), 0);
        }
        const u32x4 sk0 = ld16(shk + d0 * 4), sk1 = ld16(shk + d0 * 4 + 8);
#pragma unroll
        for (int e = 0; e < 4; ++e) {
            const unsigned w01 = (e < 2 ? sk0 : sk1)[(e & 1) * 2], w23 = (e < 2 ? sk0 : sk1)[(e & 1) * 2 + 1];
            const float w0 = bflo(w01), w1 = bfhi(w01), w2 = bflo(w23), w3 = bfhi(w23);
            float kk[11], u[8];
#pragma unroll
            for (int j = 0; j < 11; ++j) kk[j] = (e & 1) ? bfhi(kr[j][e >> 1]) : bflo(kr[j][e >> 1]);
#pragma unroll
            for (int j = 0; j < 8; ++j) {
                const float a = fmaf(kk[j + 3], w0, fmaf(kk[j + 2], w1, fmaf(kk[j + 1], w2, kk[j] * w3)));
                u[j] = a * ((e & 1) ? bfhi(vr[j][e >> 1]) : bflo(vr[j][e >> 1]));
            }
            const u32x4 o = {pack2(u[7], u[6]), pack2(u[5], u[4]), pack2(u[3], u[2]), pack2(u[1], u[0])};
            __builtin_amdgcn_raw_buffer_store_b128(o, rs, WS_UR + ((d0 + e) * SEQ + (SEQ - 8) - s0) * 2, 0, 16);
        }
        publish(ws, WS_URF + ((hs * 32 + tg) * 2 + (wb4 & 1)) * FL, tag, lane);
        barrier();
    }

    {
        const int ch = wb4 >> 1, sh = wb4 & 1;
        const int d = 2 * blk + ch;
        const float dd = bf2f(ssmd[d]);
        wait_flags<24>(ws, WS_URF + ((d >> 6) * 64 + lane) * FL, true, tag);
        char* ul = smem + L_U + ch * 2 * U_RS;
        u32x4 uv[4];
#pragma unroll
        for (int j = 0; j < 4; ++j)
            uv[j] = __builtin_amdgcn_raw_buffer_load_b128(rs, WS_UR + d * 4096 + j * 1024 + lane * 16 + 2 * sh, 0, 16);
        if (sh && lane == 63) uv[3][3] &= 0xffffu;
        lds_wait((LDSP unsigned*)(smem + L_CNT), 8);
#pragma unroll
        for (int j = 0; j < 4; ++j)
            *reinterpret_cast<u32x4*>(ul + sh * U_RS + j * 1024 + lane * 16) = uv[j];
        if (lane < 2) *reinterpret_cast<u32x4*>(ul + sh * U_RS + 4096 + lane * 16) = (u32x4){0, 0, 0, 0};
        LDSP unsigned* pc = (LDSP unsigned*)(smem + L_CNT + 16 + 4 * ch);
        lds_arrive(pc, lane);
        lds_wait(pc, 2);
        const __amdgpu_buffer_rsrc_t hk = __builtin_amdgcn_make_buffer_rsrc((void*)(ssmk + (size_t)d * SEQ), 0, SEQ * 2, 0x00020000);
        if (sh) conv_wave<1, 2, 4, 7>(hk, ul, rs, WS_ZT + d * 4096, dd, lane);
        else conv_wave<0, 3, 5, 6>(hk, ul, rs, WS_ZT + d * 4096, dd, lane);
        publish(ws, WS_ZF + (d * 2 + sh) * FL, tag, lane);
    }

    if (wb4 < 2) {
        const int c8 = lane & 7;
        const int s0 = 64 * tg + 4 * ((lane >> 3) + 8 * wb4);
        const int d0 = 64 * hs + 8 * c8;
        u32x4 qr[4];
#pragma unroll
        for (int j = 0; j < 4; ++j) qr[j] = ld16(bq + (size_t)(s0 + j) * DM + d0);
        const int sh = (0x96 >> (tg >> 2)) & 1;
        wait_flags<24>(ws, WS_ZF + ((64 * hs + lane) * 2 + sh) * FL, true, tag);
        u32x2 z[8];
#pragma unroll
        for (int e = 0; e < 8; ++e)
            z[e] = __builtin_amdgcn_raw_buffer_load_b64(rs, WS_ZT + ((d0 + e) * SEQ + s0) * 2, 0, 16);
#pragma unroll
        for (int i = 0; i < 4; ++i) {
            u32x4 o;
#pragma unroll
            for (int j = 0; j < 4; ++j) {
                const unsigned za = z[2 * j][i >> 1], zb = z[2 * j + 1][i >> 1];
                const float a = (i & 1) ? bfhi(za) : bflo(za);
                const float b = (i & 1) ? bfhi(zb) : bflo(zb);
                o[j] = pack2(bflo(qr[i][j]) * a, bfhi(qr[i][j]) * b);
            }
            *reinterpret_cast<u32x4*>(out + (size_t)(s0 + i) * 1024 + 512 + d0) = o;
        }
    } else if (blk == 0 && wb4 == 2) {
#pragma unroll 1
        for (int j = 0; j < 8; ++j) wait_flags<24>(ws, WS_URF + (j * 64 + lane) * FL, true, tag);
        if (lane == 0) __hip_atomic_store((GLBP u64*)(ws + WS_EPOCH), tag, __ATOMIC_RELAXED, __HIP_MEMORY_SCOPE_AGENT);
    }

    u32x4 fe[8], fo[8];
    {
        const __amdgpu_buffer_rsrc_t wr = __builtin_amdgcn_make_buffer_rsrc((void*)wo, 0, DM * DM * 2, 0x00020000);
        const int vo = (4 * h * DM + 2 * (r ^ (8 * wb4))) * 2;
        const int so = (128 * wb4 * DM + 64 * hs) * 2;
        unsigned wd[8][8];
#pragma unroll
        for (int s = 0; s < 8; ++s)
#pragma unroll
            for (int j = 0; j < 8; ++j)
                wd[s][j] = __builtin_amdgcn_raw_buffer_load_b32(wr, vo + (j & 3) * 1024, so + (16 * s + 8 * (j >> 2)) * 1024, 0);
        __builtin_amdgcn_sched_barrier(0);
#pragma unroll
        for (int s = 0; s < 8; ++s) {
#pragma unroll
            for (int j = 0; j < 4; ++j) {
                fe[s][j] = __builtin_amdgcn_perm(wd[s][2 * j + 1], wd[s][2 * j], 0x05040100u);
                fo[s][j] = __builtin_amdgcn_perm(wd[s][2 * j + 1], wd[s][2 * j], 0x07060302u);
            }
            asm volatile("" : "+v"(fe[s]), "+v"(fo[s]));
        }
    }
    LDSP unsigned* cnt = (LDSP unsigned*)(smem + L_CNT + 32);
    lds_arrive(cnt, lane);
    while (__hip_atomic_load((LDSP unsigned*)(smem + L_CNT + 8), __ATOMIC_RELAXED, __HIP_MEMORY_SCOPE_WORKGROUP) < 4u)
        __builtin_amdgcn_s_sleep(8);
#pragma unroll 1
    for (int i = 0; i < 2; ++i) {
        wait_flags<1>(ws, WS_OFL + ((tg * 8 + 2 * wb4 + (lane >> 2)) * 8 + 4 * i + (lane & 3)) * FL, lane < 8, tag);
        u32x4 of[8];
#pragma unroll
        for (int s = 0; s < 8; ++s)
            of[s] = __builtin_amdgcn_raw_buffer_load_b128(
                rs, lane * 16 + (s & 3) * 1024, WS_OF + ((2 * tg + i) * 32 + 8 * wb4 + (s & 4)) * 1024, 16);
        f32x16 ae = {0}, ao = {0};
#pragma unroll
        for (int s = 0; s < 8; ++s) {
            ae = mfma32(fe[s], of[s], ae);
            ao = mfma32(fo[s], of[s], ao);
        }
        lds_wait(cnt, 4 + 8 * i);
#pragma unroll
        for (int g = 1; g < 4; ++g) {
            f32x4* p = reinterpret_cast<f32x4*>(smem + L_RED + ((g ^ wb4) * 3 + g - 1) * 2048) + lane;
            p[0] = (f32x4){ae[4 * g], ae[4 * g + 1], ae[4 * g + 2], ae[4 * g + 3]};
            p[64] = (f32x4){ao[4 * g], ao[4 * g + 1], ao[4 * g + 2], ao[4 * g + 3]};
        }
        lds_arrive(cnt, lane);
        lds_wait(cnt, 8 + 8 * i);
        f32x4 e = {ae[0], ae[1], ae[2], ae[3]}, o = {ao[0], ao[1], ao[2], ao[3]};
        const f32x4* p = reinterpret_cast<const f32x4*>(smem + L_RED + wb4 * 3 * 2048) + lane;
#pragma unroll
        for (int g = 0; g < 3; ++g) {
            e += p[g * 128];
            o += p[g * 128 + 64];
        }
        const u32x4 res = {pack2(e[0], o[0]), pack2(e[1], o[1]), pack2(e[2], o[2]), pack2(e[3], o[3])};
        *reinterpret_cast<u32x4*>(out + (size_t)(64 * tg + 32 * i + r) * 1024 + 64 * hs + 16 * wb4 + 8 * h) = res;
        lds_arrive(cnt, lane);
    }
}

__global__ void nop_kernel(char* ws) { if (ws == nullptr) __builtin_trap(); }
extern "C" void kernel_launch(void* const* d_in, const int* in_sizes, int n_in, void* d_out,
                              int out_size, void* d_ws, size_t ws_size, hipStream_t stream) {
    (void)hipFuncSetAttribute((const void*)fused_kernel, hipFuncAttributeMaxDynamicSharedMemorySize, LDS_BYTES);
    fused_kernel<<<256, 768, LDS_BYTES, stream>>>(
        (const u16*)d_in[0], (const u16*)d_in[1], (const u16*)d_in[2], (const u16*)d_in[3],
        (const u16*)d_in[4], (const u16*)d_in[5], (const u16*)d_in[6], (const u16*)d_in[7],
        (const u16*)d_in[8], (const u16*)d_in[9], (const u16*)d_in[10], (u16*)d_out, (char*)d_ws);
    nop_kernel<<<1, 64, 0, stream>>>((char*)d_ws);
}
```

```cpp
#include <hip/hip_runtime.h>
#include <hip/hip_bf16.h>
#define OUT_AUX 0
#define CSP 3

typedef __attribute__((ext_vector_type(8))) __bf16 bf16x8;
typedef __attribute__((ext_vector_type(2))) __bf16 bf16x2;
typedef __attribute__((ext_vector_type(16))) float f32x16;
typedef __attribute__((ext_vector_type(4))) float f32x4;
typedef __attribute__((ext_vector_type(2))) float f32x2;
typedef __attribute__((ext_vector_type(4))) unsigned u32x4;
typedef __attribute__((ext_vector_type(2))) unsigned u32x2;
typedef __attribute__((ext_vector_type(4))) short s16x4;
typedef unsigned long long u64;
typedef unsigned short u16;
#define LDSP __attribute__((address_space(3)))
#define GLBP __attribute__((address_space(1)))

constexpr int SEQ = 2048;
constexpr int DM = 512;

constexpr int FL = 128;
constexpr int WS_EPOCH = 0;
constexpr int KVF_REP = 128 * FL + 4096;
constexpr int WS_KVF = 4096;
constexpr int WS_URF = WS_KVF + 8 * KVF_REP;
constexpr int WS_ZF = WS_URF + 256 * FL;
constexpr int WS_OFL = WS_ZF + 1024 * FL;
constexpr int WS_KV = WS_OFL + 2048 * FL;
constexpr int WS_KVG = WS_KV + 131072;
constexpr int WS_UR = WS_KVG + 262144;
constexpr int WS_ZT = WS_UR + 2097152 + 4096;
constexpr int WS_OF = WS_ZT + 2097152;
constexpr int WS_END = WS_OF + 2097152;

constexpr int L_X = 0;
constexpr int L_W = 65536;
constexpr int L_K = 0;
constexpr int L_V = 65536;
constexpr int L_CONST = 131072;
constexpr int L_CNT = L_CONST + 9 * 512;
constexpr int L_U = L_CNT + 128;
constexpr int U_RS = 4160;
constexpr int L_RED = L_U;
constexpr int L_QX = 147456;
constexpr int LDS_BYTES = 163840;
constexpr int C_FIRST = 0;
constexpr int C_MID = 4;
constexpr int C_NEAR = 8;
constexpr int C_BIN = 12;
constexpr int C_U = 16;
constexpr int C_KVRD = 24;
constexpr int C_AIN = 28;
constexpr int C_TAIL = 32;
constexpr int C_GATE = 36;
constexpr int C_LAST = 40;
constexpr int C_NEXT = 44;
constexpr int C_SEEN = 48;
constexpr int C_UR = 52;

constexpr float QSCALE = 0.25f * 1.4426950408889634f;
constexpr unsigned SPIN_CAP = 1u << 18;

__device__ __forceinline__ float bf2f(u16 v) { return __uint_as_float(((unsigned)v) << 16); }
__device__ __forceinline__ float bflo(unsigned v) { return __uint_as_float(v << 16); }
__device__ __forceinline__ float bfhi(unsigned v) { return __uint_as_float(v & 0xffff0000u); }
__device__ __forceinline__ unsigned pack2(float lo, float hi) {
    f32x2 f = {lo, hi};
    bf16x2 b = __builtin_convertvector(f, bf16x2);
    return __builtin_bit_cast(unsigned, b);
}
__device__ __forceinline__ float rbf(float x) { return (float)(__bf16)x; }
__device__ __forceinline__ bf16x8 as_bf(u32x4 v) { return __builtin_bit_cast(bf16x8, v); }
__device__ __forceinline__ f32x16 mfma32(u32x4 a, u32x4 b, f32x16 c) {
    return __builtin_amdgcn_mfma_f32_32x32x16_bf16(as_bf(a), as_bf(b), c, 0, 0, 0);
}
__device__ __forceinline__ f32x4 mfma16(u32x4 a, u32x4 b, f32x4 c) {
    return __builtin_amdgcn_mfma_f32_16x16x32_bf16(as_bf(a), as_bf(b), c, 0, 0, 0);
}
__device__ __forceinline__ u32x4 ld16(const void* p) { return *reinterpret_cast<const u32x4*>(p); }
__device__ __forceinline__ u32x2 ld8(const void* p) { return *reinterpret_cast<const u32x2*>(p); }
__device__ __forceinline__ u32x4 tr_frag(const char* p, int second) {
    const s16x4 a = __builtin_amdgcn_ds_read_tr16_b64_v4i16((LDSP s16x4*)p);
    const s16x4 b = __builtin_amdgcn_ds_read_tr16_b64_v4i16((LDSP s16x4*)(p + second));
    const u32x2 a2 = __builtin_bit_cast(u32x2, a), b2 = __builtin_bit_cast(u32x2, b);
    return (u32x4){a2[0], a2[1], b2[0], b2[1]};
}
__device__ __forceinline__ void dma16(const void* src, unsigned voff, unsigned dst) {
    asm volatile("s_mov_b32 m0, %2\n\ts_nop 0\n\tglobal_load_lds_dwordx4 %0, %1" ::"v"(voff), "s"(src), "s"(dst) : "memory");
}
__device__ __forceinline__ void dma16_dev(const void* src, unsigned voff, unsigned dst) {
    asm volatile("s_mov_b32 m0, %2\n\ts_nop 0\n\tglobal_load_lds_dwordx4 %0, %1 sc1" ::"v"(voff), "s"(src), "s"(dst) : "memory");
}
__device__ __forceinline__ void barrier() {
    asm volatile("s_waitcnt lgkmcnt(0)\n\ts_barrier" ::: "memory");
}
__device__ __forceinline__ void drain() { asm volatile("s_waitcnt vmcnt(0)" ::: "memory"); }

__device__ __forceinline__ void lds_arrive(unsigned addr) {
    u64 sx;
    asm volatile("s_waitcnt lgkmcnt(0)\n\ts_mov_b64 %0, exec\n\ts_mov_b64 exec, 1\n\t"
                 "ds_add_u32 %1, %2\n\ts_mov_b64 exec, %0"
                 : "=&s"(sx) : "v"(addr), "v"(1u) : "memory");
}
__device__ __forceinline__ unsigned lds_arrive_count(unsigned addr) {
    u64 sx;
    unsigned v, st;
    asm volatile("s_waitcnt lgkmcnt(0)\n\ts_mov_b64 %0, exec\n\ts_mov_b64 exec, 1\n\t"
                 "ds_add_rtn_u32 %1, %3, %4\n\ts_waitcnt lgkmcnt(0)\n\tv_readfirstlane_b32 %2, %1\n\ts_mov_b64 exec, %0"
                 : "=&s"(sx), "=&v"(v), "=&s"(st) : "v"(addr), "v"(1u) : "memory");
    return st;
}
template <int SLEEP>
__device__ __forceinline__ void lds_wait(unsigned addr, unsigned n) {
    unsigned v, st;
    asm volatile("s_branch 2f\n1:\n\ts_sleep %4\n2:\n\tds_read_b32 %0, %2\n\ts_waitcnt lgkmcnt(0)\n\t"
                 "v_readfirstlane_b32 %1, %0\n\ts_cmp_lt_u32 %1, %3\n\ts_cbranch_scc1 1b"
                 : "=&v"(v), "=&s"(st) : "v"(addr), "s"(n), "i"(SLEEP) : "scc", "memory");
}

__device__ __forceinline__ void yield_until(unsigned addr, unsigned n) {
    unsigned v, st;
    asm volatile("ds_read_b32 %0, %2\n\ts_setprio 3\n\ts_waitcnt lgkmcnt(0)\n\tv_readfirstlane_b32 %1, %0\n\t"
                 "s_cmp_lt_u32 %1, %3\n\ts_cbranch_scc0 1f\n\ts_setprio 1\n1:"
                 : "=&v"(v), "=&s"(st) : "v"(addr), "s"(n) : "scc", "memory");
}

__device__ __forceinline__ void publish(char* ws, int off, u64 tag, int lane) {
    drain();
    if (lane == 0) __hip_atomic_store((GLBP u64*)(ws + off), tag, __ATOMIC_RELAXED, __HIP_MEMORY_SCOPE_AGENT);
}
template <int SLEEP>
__device__ __forceinline__ void wait_flags(char* ws, int off, bool active, u64 tag) {
#pragma unroll 1
    for (unsigned spin = 0; spin < SPIN_CAP; ++spin) {
        u64 v = tag;
        if (active) v = __hip_atomic_load((GLBP u64*)(ws + off), __ATOMIC_RELAXED, __HIP_MEMORY_SCOPE_AGENT);
        if (__all(v == tag)) break;
        __builtin_amdgcn_s_sleep(SLEEP);
    }
    asm volatile("" ::: "memory");
}

#define STR2(x) #x
#define STR(x) STR2(x)
#define EX(r) "v_exp_f32 v" STR(r) ", v" STR(r) "\n"
#define CV(d, a, b) "v_cvt_pk_bf16_f32 v" STR(d) ", v" STR(a) ", v" STR(b) "\n"
#define TILE(x0, x1, x2, x3, x4, x5, x6, x7, x8, x9, x10, x11, x12, x13, x14, x15, NEXT, u)     \
    "s_waitcnt lgkmcnt(2)\n"                                                                    \
    "v_mfma_f32_32x32x16_bf16 " NEXT ", v[104:107], %[q], v[116:131]\n"                         \
    "ds_read_b128 v[104:107], %[ka] offset:" STR((u + 2) * 1024) "\n"                           \
    EX(x0) EX(x1) EX(x2) EX(x3) CV(96, x0, x1) EX(x4) EX(x5) CV(97, x2, x3)                     \
    "s_waitcnt lgkmcnt(2)\n"                                                                    \
    "v_mfma_f32_32x32x16_bf16 %[o], v[112:115], v[100:103], %[o]\n"                             \
    "ds_read_b128 v[112:115], %[va] offset:" STR((2 * u + 1) * 512) "\n"                        \
    EX(x6) EX(x7) CV(98, x4, x5) EX(x8) CV(99, x6, x7) EX(x9) EX(x10) EX(x11)                   \
    "s_waitcnt lgkmcnt(2)\n"                                                                    \
    "v_mfma_f32_32x32x16_bf16 %[o], v[108:111], v[96:99], %[o]\n"                               \
    "ds_read_b128 v[108:111], %[va] offset:" STR((2 * u + 2) * 512) "\n"                        \
    EX(x12) EX(x13) CV(100, x8, x9) EX(x14) EX(x15) CV(101, x10, x11) CV(102, x12, x13)         \
    CV(103, x14, x15)
#define TILE_X(u) TILE(64, 65, 66, 67, 68, 69, 70, 71, 72, 73, 74, 75, 76, 77, 78, 79, "v[80:95]", u)
#define TILE_Y(u) TILE(80, 81, 82, 83, 84, 85, 86, 87, 88, 89, 90, 91, 92, 93, 94, 95, "v[64:79]", u)
#define MOVC(r) "v_mov_b32 v" STR(r) ", v116\n"
#define LDS_WAIT(off, n, lbl)                                                                   \
    lbl ":\n"                                                                                   \
    "ds_read_b32 v96, %[ca] offset:" STR(off) "\n"                                              \
    "s_waitcnt lgkmcnt(0)\n"                                                                    \
    "v_readfirstlane_b32 %[st], v96\n"                                                          \
    "s_cmp_lt_u32 %[st], " STR(n) "\n"                                                          \
    "s_cbranch_scc1 " lbl "b\n"
#define LDS_ARRIVE(off)                                                                         \
    "s_mov_b64 %[sx], exec\n"                                                                   \
    "s_mov_b64 exec, 1\n"                                                                       \
    "v_mov_b32 v96, 1\n"                                                                        \
    "ds_add_u32 %[ca], v96 offset:" STR(off) "\n"                                               \
    "s_mov_b64 exec, %[sx]\n"
#define ATT_CLOB                                                                                \
    "v64", "v65", "v66", "v67", "v68", "v69", "v70", "v71", "v72", "v73", "v74", "v75", "v76",  \
    "v77", "v78", "v79", "v80", "v81", "v82", "v83", "v84", "v85", "v86", "v87", "v88", "v89",  \
    "v90", "v91", "v92", "v93", "v94", "v95", "v96", "v97", "v98", "v99", "v100", "v101",       \
    "v102", "v103", "v104", "v105", "v106", "v107", "v108", "v109", "v110", "v111", "v112",     \
    "v113", "v114", "v115", "v116", "v117", "v118", "v119", "v120", "v121", "v122", "v123",     \
    "v124", "v125", "v126", "v127", "v128", "v129", "v130", "v131"

#define ITER_STAMP
#define ITER_OUT
#define ITER_CLOB
__device__ __forceinline__ f32x16 attn_pass(unsigned ka, unsigned va, unsigned vstep, unsigned ca,
                                            u32x4 q, float mgiven, int given) {
    f32x16 o = {0};
    int cnt, ev, st;
    u64 sx;
    asm volatile(
        "ds_read_b128 v[104:107], %[ka]\n"
        "v_mov_b32 v116, %[mg]\n"
        "s_cmp_lg_u32 %[gv], 0\n"
        "s_waitcnt lgkmcnt(0)\n"
        "s_cbranch_scc1 11f\n"
        "v_mfma_f32_32x32x16_bf16 v[64:79], v[104:107], %[q], 0\n"
        "s_nop 11\n"
        "v_max3_f32 v96, v64, v65, v66\n"
        "v_max3_f32 v97, v67, v68, v69\n"
        "v_max3_f32 v98, v70, v71, v72\n"
        "v_max3_f32 v99, v73, v74, v75\n"
        "v_max3_f32 v96, v96, v76, v77\n"
        "v_max3_f32 v97, v97, v78, v79\n"
        "v_max3_f32 v96, v96, v98, v99\n"
        "v_max_f32 v96, v96, v97\n"
        "v_mov_b32 v97, v96\n"
        "s_nop 1\n"
        "v_permlane32_swap_b32 v96, v97\n"
        "s_nop 1\n"
        "v_max_f32 v116, v96, v97\n"
        "11:\n"
        "v_xor_b32 v116, 0x80000000, v116\n"
        MOVC(117) MOVC(118) MOVC(119) MOVC(120) MOVC(121) MOVC(122) MOVC(123) MOVC(124)
        MOVC(125) MOVC(126) MOVC(127) MOVC(128) MOVC(129) MOVC(130) MOVC(131)
        "v_mov_b32 v100, 0\n v_mov_b32 v101, 0\n v_mov_b32 v102, 0\n v_mov_b32 v103, 0\n"
        "s_movk_i32 %[cnt], 16\n"
        "s_setprio 2\n"
        "v_mfma_f32_32x32x16_bf16 v[64:79], v[104:107], %[q], v[116:131]\n"
        "ds_read_b128 v[104:107], %[ka] offset:1024\n"
        "ds_read_b128 v[112:115], %[va]\n"
        "ds_read_b128 v[108:111], %[va]\n"
        "s_nop 10\n"
        "s_movk_i32 %[ev], 16\n"
        "12:\n"
        TILE_X(0) TILE_Y(1) TILE_X(2) TILE_Y(3)
        "v_add_u32 %[ka], 4096, %[ka]\n"
        "v_add_u32 %[va], %[vs], %[va]\n"
        ITER_STAMP
        "s_cmp_eq_u32 %[cnt], %[ev]\n"
        "s_cbranch_scc1 14f\n"
        "13:\n"
        "s_sub_u32 %[cnt], %[cnt], 1\n"
        "s_cmp_lg_u32 %[cnt], 0\n"
        "s_cbranch_scc1 12b\n"
        "s_branch 18f\n"
        "14:\n"
        "s_cmp_eq_u32 %[ev], 16\n"
        "s_cbranch_scc0 21f\n"
        LDS_WAIT(44, 2, "22")
        "s_movk_i32 %[ev], 14\n"
        "s_branch 13b\n"
        "21:\n"
        "s_cmp_eq_u32 %[ev], 14\n"
        "s_cbranch_scc0 15f\n"
        LDS_WAIT(4, 2, "17")
        "s_movk_i32 %[ev], 9\n"
        "s_branch 13b\n"
        "15:\n"
        "s_cmp_eq_u32 %[ev], 9\n"
        "s_cbranch_scc0 16f\n"
        LDS_WAIT(40, 2, "19")
        "s_setprio 1\n"
        "s_movk_i32 %[ev], 3\n"
        "s_branch 13b\n"
        "16:\n"
        "s_setprio 0\n"
        LDS_ARRIVE(8)
        "s_waitcnt lgkmcnt(0)\n"
        "s_branch 13b\n"
        "18:\n"
        "s_waitcnt lgkmcnt(0)\n"
        "v_mfma_f32_32x32x16_bf16 %[o], v[112:115], v[100:103], %[o]\n"
        "s_nop 15\n"
        : [o] "+v"(o), [ka] "+v"(ka), [va] "+v"(va), [cnt] "=&s"(cnt), [ev] "=&s"(ev), [st] "=&s"(st), [sx] "=&s"(sx) ITER_OUT
        : [q] "v"(q), [vs] "v"(vstep), [ca] "v"(ca), [mg] "v"(mgiven), [gv] "s"(given)
        : ATT_CLOB, "scc", "memory" ITER_CLOB);
    return o;
}

__device__ __forceinline__ float row_max(const char* kb, u32x4 q) {
    float m = -__builtin_inff();
    const f32x16 z = {0};
    for (int t = 0; t < 64; ++t) {
        const f32x16 s = mfma32(ld16(kb + t * 1024), q, z);
#pragma unroll
        for (int j = 0; j < 16; ++j) m = fmaxf(m, s[j]);
    }
    return fmaxf(m, __shfl_xor(m, 32));
}

#define GAP __builtin_amdgcn_sched_barrier(0); asm volatile("s_sleep 1"); __builtin_amdgcn_sched_barrier(0);
template <int T0, int T1, int T2, int T3>
__device__ __forceinline__ void conv_wave(__amdgpu_buffer_rsrc_t hr, const char* ul,
                                          __amdgpu_buffer_rsrc_t rs, int zoff, float dd, int lane) {
    constexpr int NF = T3 + 1;
    const int r = lane & 15, g = lane >> 4;
    const int ns = (SEQ - 1) - 16 * (g >> 1) - r + 8 * (g & 1);
    unsigned ap = (unsigned)(size_t)(LDSP const char*)ul + ((ns & 1) ? (U_RS + 2 * (ns - 1)) : 2 * ns) - 512 * (NF - 1);
    int b0 = 2 * (16 * r - 16 * (g >> 1) + 8 * (g & 1));
    f32x4 c0 = {0}, c1 = {0}, c2 = {0}, c3 = {0};
#pragma unroll 1
    for (int rho = 0; rho < 8; ++rho) {
        u32x4 A[NF], B[NF];
        int b1 = b0 + 512;
        asm volatile("" : "+v"(b1), "+v"(ap));
        B[0] = __builtin_amdgcn_raw_buffer_load_b128(hr, b0, 0, 0);
#pragma unroll
        for (int b = 1; b < NF; ++b) B[b] = __builtin_amdgcn_raw_buffer_load_b128(hr, b1 + 512 * (b - 1), 0, 0);
#pragma unroll
        for (int a = 0; a < NF; ++a) {
            volatile LDSP const unsigned* p = (volatile LDSP const unsigned*)(size_t)(ap + 512 * (NF - 1 - a));
            A[a] = (u32x4){p[0], p[1], p[2], p[3]};
        }
        __builtin_amdgcn_sched_barrier(0);
#pragma unroll
        for (int a = 0; a < NF; ++a) {
            c3 = mfma16(A[a], B[T3 - a], c3);
            GAP
            if (a <= T2) { c2 = mfma16(A[a], B[a <= T2 ? T2 - a : 0], c2); GAP }
            if (a <= T1) { c1 = mfma16(A[a], B[a <= T1 ? T1 - a : 0], c1); GAP }
            if (a <= T0) { c0 = mfma16(A[a], B[a <= T0 ? T0 - a : 0], c0); GAP }
        }
        b0 -= 64;
        ap -= 64;
    }
    const int ts[4] = {T0, T1, T2, T3};
    const f32x4 cs[4] = {c0, c1, c2, c3};
#pragma unroll
    for (int i = 0; i < 4; ++i) {
        const int s = 256 * ts[i] + 16 * r + 4 * g;
        const u32x2 uu = ld8(ul + 2 * ((SEQ - 4) - s));
        const u32x2 o = {pack2(rbf(cs[i][0]) + rbf(bfhi(uu[1]) * dd), rbf(cs[i][1]) + rbf(bflo(uu[1]) * dd)),
                         pack2(rbf(cs[i][2]) + rbf(bfhi(uu[0]) * dd), rbf(cs[i][3]) + rbf(bflo(uu[0]) * dd))};
        __builtin_amdgcn_raw_buffer_store_b64(o, rs, zoff + 2 * s, 0, 16);
    }
}

template <bool WFIRST>
__device__ __forceinline__ void kv_proj(const char* xb, const char* wp, int g, int c, f32x4& a0, f32x4& a1) {
    u32x4 fw[16], f0[16], f1[16];
    asm volatile("" ::: "memory");
#define KV_LOAD(G)                                                                   \
    _Pragma("unroll") for (int s = 4 * G; s < 4 * G + 4; ++s) {                      \
        const int xo = G * 256 + (((4 * (s & 3) + g) ^ c) << 4);                     \
        fw[s] = tr_frag(wp + s * 1024, 128);                                         \
        f0[s] = ld16(xb + xo);                                                       \
        f1[s] = ld16(xb + xo + 16384);                                               \
    }                                                                                \
    __builtin_amdgcn_sched_barrier(0);
#define KV_MMA(G)                                                                    \
    _Pragma("unroll") for (int s = 4 * G; s < 4 * G + 4; ++s) {                      \
        a0 = WFIRST ? mfma16(fw[s], f0[s], a0) : mfma16(f0[s], fw[s], a0);           \
        a1 = WFIRST ? mfma16(fw[s], f1[s], a1) : mfma16(f1[s], fw[s], a1);           \
    }                                                                                \
    __builtin_amdgcn_sched_barrier(0);
    KV_LOAD(0) KV_LOAD(1) KV_MMA(0) KV_LOAD(2) KV_MMA(1) KV_LOAD(3) KV_MMA(2) KV_MMA(3)
}

__global__ void __launch_bounds__(768)
fused_kernel(const u16* __restrict__ x, const u16* __restrict__ wq, const u16* __restrict__ wk,
             const u16* __restrict__ wv, const u16* __restrict__ wo, const u16* __restrict__ bq,
             const u16* __restrict__ bk, const u16* __restrict__ bv, const u16* __restrict__ shk,
             const u16* __restrict__ ssmk, const u16* __restrict__ ssmd, u16* __restrict__ out,
             char* ws) {
    extern __shared__ __attribute__((aligned(16))) char smem[];
    const int lane = threadIdx.x & 63;
    const int w = __builtin_amdgcn_readfirstlane(threadIdx.x >> 6);
    const int blk = blockIdx.x;
    const int tg = (blk & 7) * 4 + (blk >> 6);
    const int hs = (blk >> 3) & 7;
    const int r = lane & 31, h = lane >> 5;
    const __amdgpu_buffer_rsrc_t rs = __builtin_amdgcn_make_buffer_rsrc(ws, 0, WS_END, 0x00020000);
    asm volatile("" ::"s"(x), "s"(wq), "s"(wk), "s"(wv), "s"(wo), "s"(bq), "s"(bk), "s"(bv), "s"(shk),
                 "s"(ssmk), "s"(ssmd), "s"(out), "s"(ws));

    const bool kvp = hs < 2;
    const int xt = hs & 1;
    const unsigned base = (unsigned)(size_t)(LDSP char*)smem;
    const unsigned cb = base + L_CNT;
    const int rot = 8 * (blk & 7);
    const u16* xsrc = x + (size_t)(64 * tg) * DM;
    const u16* wsrc = wq + (size_t)(64 * (w & 7)) * DM + 64 * hs;
    const unsigned wvo = (lane >> 3) * 1024 + (((lane & 7) ^ (((lane >> 4) & 1) << 2)) << 4);
    if (w >= 8) {
        __builtin_amdgcn_s_setprio(3);
        const int wb4 = w - 8;
#pragma unroll
        for (int j = 0; j < 8; ++j) {
            const int row = 32 * xt + 4 * j + wb4;
            dma16(xsrc + row * DM, (lane ^ (row & 15)) << 4, base + L_X + row * 1024);
        }
        if (kvp) {
            const u16* src = (wb4 < 2 ? wk : wv) + (wb4 & 1) * 4096;
#pragma unroll
            for (int j = 0; j < 8; ++j) dma16(src + j * 512, lane * 16, base + L_X + (xt ^ 1) * 32768 + (wb4 * 8 + j) * 1024);
        }
        if (wb4 == 0) {
            const int b3 = lane / 3, p = lane - 3 * b3;
            const unsigned v = (lane < 27 && p == 0) ? 0x3f803f80u : 0u;
            const unsigned dst = lane < 27 ? base + L_CONST + b3 * 512 + (p == 2 ? 64 : 16 * p) : cb + ((lane - 27) & 7) * 16;
            asm volatile("ds_write_b128 %0, %1" ::"v"(dst), "v"((u32x4){v, v, v, v}) : "memory");
        }
    } else if (!kvp) {
#pragma unroll
        for (int j = 0; j < 4; ++j) {
            const int row = 32 * (xt ^ 1) + 8 * j + w;
            dma16(xsrc + row * DM, (lane ^ (row & 15)) << 4, base + L_X + row * 1024);
        }
#pragma unroll
        for (int j = 0; j < 8; ++j) dma16(wsrc + j * 8 * DM, wvo, base + L_W + (8 * w + j) * 1024);
    }
    u64 tag;
    asm volatile("s_load_dwordx2 %0, %1, 0x0\n\ts_waitcnt lgkmcnt(0)" : "=s"(tag) : "s"(ws) : "memory");
    tag += 1;
    barrier();

    const int tq = (lane & 15) >> 2, tp = lane & 3;
    if (w < 8) {
        __builtin_amdgcn_s_setprio(2);
        if (kvp) {
#pragma unroll
            for (int j = 0; j < 8; ++j) dma16(wsrc + j * 8 * DM, wvo, base + L_W + (8 * w + j) * 1024);
            lds_wait<1>(cb + C_KVRD, 2);
#pragma unroll
            for (int j = 0; j < 4; ++j) {
                const int row = 32 * (xt ^ 1) + 8 * j + w;
                dma16(xsrc + row * DM, (lane ^ (row & 15)) << 4, base + L_X + row * 1024);
            }
        }
        const int i = w >> 2, hd = w & 3, kh = hd & 1;
        const int chunk = 4 * (hd >> 1) + 2 * ((lane >> 4) & 1) + (tp >> 1);
        const char* wb = smem + L_W + kh * 32768 + (8 * h + tq) * 128 + ((chunk ^ ((tq >> 1) << 2)) << 4) + 8 * (tp & 1);
        const char* xa[8];
#pragma unroll
        for (int s = 0; s < 8; ++s) {
            unsigned o = L_X + (32 * i + r) * 1024 + kh * 512 + (((2 * s + h) ^ (r & 15)) << 4);
            asm volatile("" : "+v"(o));
            xa[s] = smem + o;
        }
        drain();
        lds_arrive(cb + C_AIN);
        lds_wait<0>(cb + C_AIN, 8);
        lds_wait<0>(cb + C_BIN, 4);
        f32x16 acc = {0};
        u32x4 fa[16], fb[16];
#define Q_LOAD(G)                                                                    \
    _Pragma("unroll") for (int s = 4 * G; s < 4 * G + 4; ++s) {                      \
        fa[s] = tr_frag(wb + s * 2048, 512);                                         \
        fb[s] = ld16(xa[s & 7] + (s >> 3) * 256);                                    \
    }                                                                                \
    __builtin_amdgcn_sched_barrier(0);
#define Q_MMA(G)                                                                     \
    _Pragma("unroll") for (int s = 4 * G; s < 4 * G + 4; ++s) acc = mfma32(fa[s], fb[s], acc); \
    __builtin_amdgcn_sched_barrier(0);
        Q_LOAD(0) Q_LOAD(1) Q_MMA(0) Q_LOAD(2) Q_MMA(1) Q_LOAD(3) Q_MMA(2) Q_MMA(3)
        f32x4 keep[2];
        {
            f32x4* xp = reinterpret_cast<f32x4*>(smem + L_QX + (w ^ 1) * 2048) + lane;
#pragma unroll
            for (int g = 0; g < 2; ++g) {
                f32x4 lo = {acc[4 * g], acc[4 * g + 1], acc[4 * g + 2], acc[4 * g + 3]};
                f32x4 hi = {acc[8 + 4 * g], acc[9 + 4 * g], acc[10 + 4 * g], acc[11 + 4 * g]};
                xp[g * 64] = kh ? lo : hi;
                keep[g] = kh ? hi : lo;
            }
        }
        const unsigned ka = base + L_K + lane * 16;
        const unsigned va = base + (r < 16 ? L_V + h * 256 + r * 16
                                           : L_CONST + (r == 16 ? 0 : ((r >= 20 && r < 28) ? 64 : 16)));
        const unsigned vstep = r < 16 ? 4096 : 0;
        const int goff = WS_KVG + ((w + rot) & 63) * 4096 + lane * 16;
        u32x4 gr[4];
#pragma unroll
        for (int j = 0; j < 4; ++j) gr[j] = __builtin_amdgcn_raw_buffer_load_b128(rs, goff + j * 1024, 0, 16);
        lds_arrive(cb + C_GATE);
        lds_wait<0>(cb + C_GATE, 8);
        const f32x4* xq = reinterpret_cast<const f32x4*>(smem + L_QX + w * 2048) + lane;
        const f32x4 qa = (xq[0] + keep[0]) * QSCALE, qb = (xq[64] + keep[1]) * QSCALE;
        const u32x4 q = {pack2(qa[0], qa[1]), pack2(qa[2], qa[3]), pack2(qb[0], qb[1]), pack2(qb[2], qb[3])};
#pragma unroll 1
        for (unsigned spin = 0; spin < SPIN_CAP; ++spin) {
            bool ok = true;
#pragma unroll
            for (int j = 0; j < 4; ++j) ok = ok && gr[j][1] == (unsigned)tag && gr[j][3] == (unsigned)tag;
            if (__all(ok)) break;
            __builtin_amdgcn_s_sleep(2);
#pragma unroll
            for (int j = 0; j < 4; ++j) gr[j] = __builtin_amdgcn_raw_buffer_load_b128(rs, goff + j * 1024, 0, 16);
        }
        *reinterpret_cast<u32x4*>(smem + L_K + w * 1024 + lane * 16) = (u32x4){gr[0][0], gr[0][2], gr[1][0], gr[1][2]};
        *reinterpret_cast<u32x4*>(smem + L_V + w * 1024 + lane * 16) = (u32x4){gr[2][0], gr[2][2], gr[3][0], gr[3][2]};
        lds_arrive(cb + C_FIRST);
        lds_wait<0>(cb + C_FIRST, 8);
        float m = 0.f;
        f32x16 o;
        float l;
#pragma unroll 1
        for (int given = 0; given < 2; ++given) {
            o = attn_pass(ka, va, vstep, cb, q, m, given);
            l = __shfl(o[8], r);
            float mag = 0.f;
#pragma unroll
            for (int j = 0; j < 8; ++j) mag += fabsf(o[j]);
            if (!__any(!(l < 1.0e18f) || !(mag < 3.0e38f))) break;
            m = row_max(smem + L_K + lane * 16, q);
        }
        const float inv = 1.0f / l;
        u32x4 of;
#pragma unroll
        for (int j = 0; j < 4; ++j) of[j] = pack2(o[2 * j] * inv, o[2 * j + 1] * inv);
        __builtin_amdgcn_raw_buffer_store_b128(
            of, rs, WS_OF + ((2 * tg + i) * 32 + 4 * hs + hd) * 1024 + lane * 16, 0, 16);
        publish(ws, WS_OFL + ((tg * 8 + hs) * 8 + w) * FL, tag, lane);
        return;
    }

    const int wb4 = w - 8;
    const int us0 = 64 * tg + 16 * wb4 + 8 * h, ud0 = 64 * hs + 2 * r;
    unsigned kr[11], vr[8];
    u32x4 sk;
    {
        const __amdgpu_buffer_rsrc_t kr_ = __builtin_amdgcn_make_buffer_rsrc((void*)bk, 0, SEQ * DM * 2, 0x00020000);
        const __amdgpu_buffer_rsrc_t vr_ = __builtin_amdgcn_make_buffer_rsrc((void*)bv, 0, SEQ * DM * 2, 0x00020000);
        const int o0 = (us0 * DM + ud0) * 2;
        int om = o0 - 3072;
        asm volatile("" : "+v"(om));
#pragma unroll
        for (int j = 0; j < 3; ++j) kr[j] = __builtin_amdgcn_raw_buffer_load_b32(kr_, om + 1024 * j, 0, 0);
#pragma unroll
        for (int j = 0; j < 8; ++j) {
            kr[3 + j] = __builtin_amdgcn_raw_buffer_load_b32(kr_, o0 + 1024 * (j & 3), 4096 * (j >> 2), 0);
            vr[j] = __builtin_amdgcn_raw_buffer_load_b32(vr_, o0 + 1024 * (j & 3), 4096 * (j >> 2), 0);
        }
        sk = ld16(shk + ud0 * 4);
    }
    asm volatile("s_waitcnt vmcnt(20)" ::: "memory");
    lds_arrive(cb + C_BIN);
    if (wb4 < 2) {
        if (kvp) {
            const int tt = 2 * tg + hs;
            const int c = lane & 15, g = lane >> 4;
            const char* xb = smem + L_X + (32 * hs + c) * 1024;
            const char* wp = smem + L_X + (xt ^ 1) * 32768 + wb4 * 16384 + (8 * g + tq) * 32 + 8 * tp;
            const int pos = (g & 1) * (wb4 ? 256 : 512) + c * 16, pos1 = wb4 ? 512 : 256;
            const int off = WS_KV + tt * 2048 + wb4 * 1024 + pos + (g >> 1) * 8;
            const int goff = WS_KVG + tt * 4096 + wb4 * 2048 + pos + (g >> 1) * 1024;
            f32x4 a0 = {0}, a1 = {0};
            lds_wait<0>(cb + C_BIN, 4);
            if (wb4 == 0) kv_proj<true>(xb, wp, g, c, a0, a1);
            else kv_proj<false>(xb, wp, g, c, a0, a1);
            const u32x2 d0 = {pack2(a0[0], a0[1]), pack2(a0[2], a0[3])}, d1 = {pack2(a1[0], a1[1]), pack2(a1[2], a1[3])};
            const unsigned tg32 = (unsigned)tag;
            __builtin_amdgcn_raw_buffer_store_b128((u32x4){d0[0], tg32, d0[1], tg32}, rs, goff, 0, 16);
            __builtin_amdgcn_raw_buffer_store_b128((u32x4){d1[0], tg32, d1[1], tg32}, rs, goff + pos1, 0, 16);
            __builtin_amdgcn_raw_buffer_store_b64(d0, rs, off, 0, 16);
            __builtin_amdgcn_raw_buffer_store_b64(d1, rs, off + pos1, 0, 16);
            lds_arrive(cb + C_KVRD);
            drain();
            if (lane < 8)
                __hip_atomic_store((GLBP u64*)(ws + WS_KVF + lane * KVF_REP + (tt * 2 + wb4) * FL), tag,
                                   __ATOMIC_RELAXED, __HIP_MEMORY_SCOPE_AGENT);
        }
    }
    {
        __builtin_amdgcn_s_setprio(1);
#pragma unroll
        for (int e = 0; e < 2; ++e) {
            const float w0 = bflo(sk[2 * e]), w1 = bfhi(sk[2 * e]), w2 = bflo(sk[2 * e + 1]), w3 = bfhi(sk[2 * e + 1]);
            float kk[11], u[8];
#pragma unroll
            for (int j = 0; j < 11; ++j) kk[j] = e ? bfhi(kr[j]) : bflo(kr[j]);
#pragma unroll
            for (int j = 0; j < 8; j += 2) {
                const unsigned p3 = pack2(kk[j] * w3, kk[j + 1] * w3), p2 = pack2(kk[j + 1] * w2, kk[j + 2] * w2);
                const unsigned p1 = pack2(kk[j + 2] * w1, kk[j + 3] * w1), p0 = pack2(kk[j + 3] * w0, kk[j + 4] * w0);
                unsigned a = pack2(bflo(p3) + bflo(p2), bfhi(p3) + bfhi(p2));
                a = pack2(bflo(a) + bflo(p1), bfhi(a) + bfhi(p1));
                a = pack2(bflo(a) + bflo(p0), bfhi(a) + bfhi(p0));
                u[j] = bflo(a) * (e ? bfhi(vr[j]) : bflo(vr[j]));
                u[j + 1] = bfhi(a) * (e ? bfhi(vr[j + 1]) : bflo(vr[j + 1]));
            }
            const u32x4 o = {pack2(u[7], u[6]), pack2(u[5], u[4]), pack2(u[3], u[2]), pack2(u[1], u[0])};
            __builtin_amdgcn_raw_buffer_store_b128(o, rs, WS_UR + ((ud0 + e) * SEQ + (SEQ - 8) - us0) * 2, 0, 16);
            yield_until(cb + C_GATE, 8);
        }
        __builtin_amdgcn_s_setprio(3);
        drain();
        if (lds_arrive_count(cb + C_UR) == 3 && lane == 0)
            __hip_atomic_store((GLBP u64*)(ws + WS_URF + (hs * 32 + tg) * FL), tag, __ATOMIC_RELAXED, __HIP_MEMORY_SCOPE_AGENT);
    }
    if (wb4 < 2) {
        lds_wait<1>(cb + C_GATE, 8);
        wait_flags<4>(ws, WS_KVF + (blk & 7) * KVF_REP + (wb4 * 64 + lane) * FL, true, tag);
        lds_arrive(cb + C_SEEN);
    }
    {
        lds_wait<1>(cb + C_SEEN, 2);
        const int s0 = wb4 < 2 ? 8 : 40, ns = wb4 < 2 ? 32 : 24;
        const char* src = ws + WS_KV + (wb4 & 1) * 1024;
        const unsigned dst = base + ((wb4 & 1) ? L_V : L_K);
#pragma unroll 1
        for (int t = s0; t < s0 + ns; ++t) dma16_dev(src + ((t + rot) & 63) * 2048, lane * 16, dst + t * 1024);
        if (wb4 < 2) {
            asm volatile("s_waitcnt vmcnt(24)" ::: "memory");
            lds_arrive(cb + C_NEXT);
        }
        drain();
        lds_arrive(cb + (wb4 < 2 ? C_MID : C_LAST));
        lds_wait<1>(cb + C_FIRST, 8);
    }

    {
        const int ch = wb4 >> 1, sh = wb4 & 1;
        const int d = 2 * blk + ch;
        const float dd = bf2f(ssmd[d]);
        wait_flags<24>(ws, WS_URF + ((d >> 6) * 32 + lane) * FL, lane < 32, tag);
        char* ul = smem + L_U + ch * 2 * U_RS;
        u32x4 uv[4];
#pragma unroll
        for (int j = 0; j < 4; ++j)
            uv[j] = __builtin_amdgcn_raw_buffer_load_b128(rs, WS_UR + d * 4096 + j * 1024 + lane * 16 + 2 * sh, 0, 16);
        if (sh && lane == 63) uv[3][3] &= 0xffffu;
#pragma unroll
        for (int j = 0; j < 4; ++j)
            *reinterpret_cast<u32x4*>(ul + sh * U_RS + j * 1024 + lane * 16) = uv[j];
        if (lane < 2) *reinterpret_cast<u32x4*>(ul + sh * U_RS + 4096 + lane * 16) = (u32x4){0, 0, 0, 0};
        lds_arrive(cb + C_U + 4 * ch);
        lds_wait<1>(cb + C_U + 4 * ch, 2);
        const __amdgpu_buffer_rsrc_t hk = __builtin_amdgcn_make_buffer_rsrc((void*)(ssmk + (size_t)d * SEQ), 0, SEQ * 2, 0x00020000);
        if (sh) conv_wave<1, 2, 4, 7>(hk, ul, rs, WS_ZT + d * 4096, dd, lane);
        else conv_wave<0, 3, 5, 6>(hk, ul, rs, WS_ZT + d * 4096, dd, lane);
        publish(ws, WS_ZF + (d * 2 + sh) * FL, tag, lane);
    }

    const __amdgpu_buffer_rsrc_t ro = __builtin_amdgcn_make_buffer_rsrc(out, 0, SEQ * 1024 * 2, 0x00020000);
    if (wb4 < 2) {
        const int c8 = lane & 7;
        const int s0 = 64 * tg + 4 * ((lane >> 3) + 8 * wb4);
        const int d0 = 64 * hs + 8 * c8;
        u32x4 qr[4];
#pragma unroll
        for (int j = 0; j < 4; ++j) qr[j] = ld16(bq + (size_t)(s0 + j) * DM + d0);
        const int sh = (0x96 >> (tg >> 2)) & 1;
        wait_flags<24>(ws, WS_ZF + ((64 * hs + lane) * 2 + sh) * FL, true, tag);
        u32x2 z[8];
#pragma unroll
        for (int e = 0; e < 8; ++e)
            z[e] = __builtin_amdgcn_raw_buffer_load_b64(rs, WS_ZT + ((d0 + e) * SEQ + s0) * 2, 0, 16);
#pragma unroll
        for (int i = 0; i < 4; ++i) {
            u32x4 o;
#pragma unroll
            for (int j = 0; j < 4; ++j) {
                const unsigned za = z[2 * j][i >> 1], zb = z[2 * j + 1][i >> 1];
                const float a = (i & 1) ? bfhi(za) : bflo(za);
                const float b = (i & 1) ? bfhi(zb) : bflo(zb);
                o[j] = pack2(bflo(qr[i][j]) * a, bfhi(qr[i][j]) * b);
            }
            __builtin_amdgcn_raw_buffer_store_b128(o, ro, ((s0 + i) * 1024 + 512 + d0) * 2, 0, OUT_AUX);
        }
    } else if (blk == 0 && wb4 == 2) {
#pragma unroll 1
        for (int j = 0; j < 4; ++j) wait_flags<24>(ws, WS_URF + (j * 64 + lane) * FL, true, tag);
        if (lane == 0) __hip_atomic_store((GLBP u64*)(ws + WS_EPOCH), tag, __ATOMIC_RELAXED, __HIP_MEMORY_SCOPE_AGENT);
    }

    u32x4 fe[8], fo[8];
    {
        const __amdgpu_buffer_rsrc_t wr = __builtin_amdgcn_make_buffer_rsrc((void*)wo, 0, DM * DM * 2, 0x00020000);
        const int vo = (4 * h * DM + 2 * (r ^ (8 * wb4))) * 2;
        const int so = (128 * wb4 * DM + 64 * hs) * 2;
        unsigned wd[8][8];
#pragma unroll
        for (int s = 0; s < 8; ++s)
#pragma unroll
            for (int j = 0; j < 8; ++j)
                wd[s][j] = __builtin_amdgcn_raw_buffer_load_b32(wr, vo + (j & 3) * 1024, so + (16 * s + 8 * (j >> 2)) * 1024, 0);
        __builtin_amdgcn_sched_barrier(0);
#pragma unroll
        for (int s = 0; s < 8; ++s) {
#pragma unroll
            for (int j = 0; j < 4; ++j) {
                fe[s][j] = __builtin_amdgcn_perm(wd[s][2 * j + 1], wd[s][2 * j], 0x05040100u);
                fo[s][j] = __builtin_amdgcn_perm(wd[s][2 * j + 1], wd[s][2 * j], 0x07060302u);
            }
            asm volatile("" : "+v"(fe[s]), "+v"(fo[s]));
        }
    }
    lds_arrive(cb + C_TAIL);
    lds_wait<8>(cb + C_NEAR, 4);
#pragma unroll 1
    for (int i = 0; i < 2; ++i) {
        wait_flags<1>(ws, WS_OFL + ((tg * 8 + 2 * wb4 + (lane >> 2)) * 8 + 4 * i + (lane & 3)) * FL, lane < 8, tag);
        u32x4 of[8];
#pragma unroll
        for (int s = 0; s < 8; ++s)
            of[s] = __builtin_amdgcn_raw_buffer_load_b128(
                rs, lane * 16 + (s & 3) * 1024, WS_OF + ((2 * tg + i) * 32 + 8 * wb4 + (s & 4)) * 1024, 16);
        __builtin_amdgcn_sched_barrier(0);
        f32x16 ae = {0}, ao = {0};
#pragma unroll
        for (int s = 0; s < 8; ++s) {
            ae = mfma32(fe[s], of[s], ae);
            ao = mfma32(fo[s], of[s], ao);
        }
        lds_wait<0>(cb + C_TAIL, 4 + 8 * i);
#pragma unroll
        for (int g = 1; g < 4; ++g) {
            f32x4* p = reinterpret_cast<f32x4*>(smem + L_RED + ((g ^ wb4) * 3 + g - 1) * 2048) + lane;
            p[0] = (f32x4){ae[4 * g], ae[4 * g + 1], ae[4 * g + 2], ae[4 * g + 3]};
            p[64] = (f32x4){ao[4 * g], ao[4 * g + 1], ao[4 * g + 2], ao[4 * g + 3]};
        }
        lds_arrive(cb + C_TAIL);
        lds_wait<0>(cb + C_TAIL, 8 + 8 * i);
        f32x4 e = {ae[0], ae[1], ae[2], ae[3]}, o = {ao[0], ao[1], ao[2], ao[3]};
        const f32x4* p = reinterpret_cast<const f32x4*>(smem + L_RED + wb4 * 3 * 2048) + lane;
#pragma unroll
        for (int g = 0; g < 3; ++g) {
            e += p[g * 128];
            o += p[g * 128 + 64];
        }
        const u32x4 res = {pack2(e[0], o[0]), pack2(e[1], o[1]), pack2(e[2], o[2]), pack2(e[3], o[3])};
        __builtin_amdgcn_raw_buffer_store_b128(res, ro, ((64 * tg + 32 * i + r) * 1024 + 64 * hs + 16 * wb4 + 8 * h) * 2, 0, OUT_AUX);
        lds_arrive(cb + C_TAIL);
    }
}

extern "C" void kernel_launch(void* const* d_in, const int* in_sizes, int n_in, void* d_out,
                              int out_size, void* d_ws, size_t ws_size, hipStream_t stream) {
    (void)hipFuncSetAttribute((const void*)fused_kernel, hipFuncAttributeMaxDynamicSharedMemorySize, LDS_BYTES);
    fused_kernel<<<256, 768, LDS_BYTES, stream>>>(
        (const u16*)d_in[0], (const u16*)d_in[1], (const u16*)d_in[2], (const u16*)d_in[3],
        (const u16*)d_in[4], (const u16*)d_in[5], (const u16*)d_in[6], (const u16*)d_in[7],
        (const u16*)d_in[8], (const u16*)d_in[9], (const u16*)d_in[10], (u16*)d_out, (char*)d_ws);
}
```

```cpp
#include <hip/hip_runtime.h>
#include <hip/hip_bf16.h>
#define OUT_AUX 0
#define CSP 3

typedef __attribute__((ext_vector_type(8))) __bf16 bf16x8;
typedef __attribute__((ext_vector_type(2))) __bf16 bf16x2;
typedef __attribute__((ext_vector_type(16))) float f32x16;
typedef __attribute__((ext_vector_type(4))) float f32x4;
typedef __attribute__((ext_vector_type(2))) float f32x2;
typedef __attribute__((ext_vector_type(4))) unsigned u32x4;
typedef __attribute__((ext_vector_type(2))) unsigned u32x2;
typedef __attribute__((ext_vector_type(4))) short s16x4;
typedef unsigned long long u64;
typedef unsigned short u16;
#define LDSP __attribute__((address_space(3)))
#define GLBP __attribute__((address_space(1)))

constexpr int SEQ = 2048;
constexpr int DM = 512;

constexpr int FL = 128;
constexpr int WS_EPOCH = 0;
constexpr int KVF_REP = 128 * FL + 4096;
constexpr int WS_KVF = 4096;
constexpr int WS_URF = WS_KVF + 8 * KVF_REP;
constexpr int WS_ZF = WS_URF + 256 * FL;
constexpr int WS_OFL = WS_ZF + 1024 * FL;
constexpr int WS_KV = WS_OFL + 2048 * FL;
constexpr int WS_KVG = WS_KV + 131072;
constexpr int WS_UR = WS_KVG + 262144;
constexpr int WS_ZT = WS_UR + 2097152 + 4096;
constexpr int WS_OF = WS_ZT + 2097152;
constexpr int WS_END = WS_OF + 2097152;

constexpr int L_X = 0;
constexpr int L_W = 65536;
constexpr int L_WKV = 147456;
constexpr int L_K = 0;
constexpr int L_V = 65536;
constexpr int L_CONST = 131072;
constexpr int L_CNT = L_CONST + 9 * 512;
constexpr int L_U = L_CNT + 128;
constexpr int U_RS = 4160;
constexpr int L_RED = L_U;
constexpr int L_QX = 147456;
constexpr int LDS_BYTES = 163840;
constexpr int C_FIRST = 0;
constexpr int C_MID = 4;
constexpr int C_NEAR = 8;
constexpr int C_BIN = 12;
constexpr int C_U = 16;
constexpr int C_KVRD = 24;
constexpr int C_TAG = 60;
constexpr int C_KVST = 56;
constexpr int C_AIN = 28;
constexpr int C_TAIL = 32;
constexpr int C_GATE = 36;
constexpr int C_LAST = 40;
constexpr int C_NEXT = 44;
constexpr int C_SEEN = 48;
constexpr int C_UR = 52;

constexpr float QSCALE = 0.25f * 1.4426950408889634f;
constexpr unsigned SPIN_CAP = 1u << 18;

__device__ __forceinline__ float bf2f(u16 v) { return __uint_as_float(((unsigned)v) << 16); }
__device__ __forceinline__ float bflo(unsigned v) { return __uint_as_float(v << 16); }
__device__ __forceinline__ float bfhi(unsigned v) { return __uint_as_float(v & 0xffff0000u); }
__device__ __forceinline__ unsigned pack2(float lo, float hi) {
    f32x2 f = {lo, hi};
    bf16x2 b = __builtin_convertvector(f, bf16x2);
    return __builtin_bit_cast(unsigned, b);
}
__device__ __forceinline__ float rbf(float x) { return (float)(__bf16)x; }
__device__ __forceinline__ bf16x8 as_bf(u32x4 v) { return __builtin_bit_cast(bf16x8, v); }
__device__ __forceinline__ f32x16 mfma32(u32x4 a, u32x4 b, f32x16 c) {
    return __builtin_amdgcn_mfma_f32_32x32x16_bf16(as_bf(a), as_bf(b), c, 0, 0, 0);
}
__device__ __forceinline__ f32x4 mfma16(u32x4 a, u32x4 b, f32x4 c) {
    return __builtin_amdgcn_mfma_f32_16x16x32_bf16(as_bf(a), as_bf(b), c, 0, 0, 0);
}
__device__ __forceinline__ u32x4 ld16(const void* p) { return *reinterpret_cast<const u32x4*>(p); }
__device__ __forceinline__ u32x2 ld8(const void* p) { return *reinterpret_cast<const u32x2*>(p); }
__device__ __forceinline__ u32x4 tr_frag(const char* p, int second) {
    const s16x4 a = __builtin_amdgcn_ds_read_tr16_b64_v4i16((LDSP s16x4*)p);
    const s16x4 b = __builtin_amdgcn_ds_read_tr16_b64_v4i16((LDSP s16x4*)(p + second));
    const u32x2 a2 = __builtin_bit_cast(u32x2, a), b2 = __builtin_bit_cast(u32x2, b);
    return (u32x4){a2[0], a2[1], b2[0], b2[1]};
}
__device__ __forceinline__ void dma16(const void* src, unsigned voff, unsigned dst) {
    asm volatile("s_mov_b32 m0, %2\n\ts_nop 0\n\tglobal_load_lds_dwordx4 %0, %1" ::"v"(voff), "s"(src), "s"(dst) : "memory");
}
__device__ __forceinline__ void dma16_dev(const void* src, unsigned voff, unsigned dst) {
    asm volatile("s_mov_b32 m0, %2\n\ts_nop 0\n\tglobal_load_lds_dwordx4 %0, %1 sc1" ::"v"(voff), "s"(src), "s"(dst) : "memory");
}
__device__ __forceinline__ void barrier() {
    asm volatile("s_waitcnt lgkmcnt(0)\n\ts_barrier" ::: "memory");
}
__device__ __forceinline__ void drain() { asm volatile("s_waitcnt vmcnt(0)" ::: "memory"); }

__device__ __forceinline__ void lds_arrive(unsigned addr) {
    u64 sx;
    asm volatile("s_waitcnt lgkmcnt(0)\n\ts_mov_b64 %0, exec\n\ts_mov_b64 exec, 1\n\t"
                 "ds_add_u32 %1, %2\n\ts_mov_b64 exec, %0"
                 : "=&s"(sx) : "v"(addr), "v"(1u) : "memory");
}
__device__ __forceinline__ unsigned lds_arrive_count(unsigned addr) {
    u64 sx;
    unsigned v, st;
    asm volatile("s_waitcnt lgkmcnt(0)\n\ts_mov_b64 %0, exec\n\ts_mov_b64 exec, 1\n\t"
                 "ds_add_rtn_u32 %1, %3, %4\n\ts_waitcnt lgkmcnt(0)\n\tv_readfirstlane_b32 %2, %1\n\ts_mov_b64 exec, %0"
                 : "=&s"(sx), "=&v"(v), "=&s"(st) : "v"(addr), "v"(1u) : "memory");
    return st;
}
template <int SLEEP>
__device__ __forceinline__ void lds_wait(unsigned addr, unsigned n) {
    unsigned v, st;
    asm volatile("s_branch 2f\n1:\n\ts_sleep %4\n2:\n\tds_read_b32 %0, %2\n\ts_waitcnt lgkmcnt(0)\n\t"
                 "v_readfirstlane_b32 %1, %0\n\ts_cmp_lt_u32 %1, %3\n\ts_cbranch_scc1 1b"
                 : "=&v"(v), "=&s"(st) : "v"(addr), "s"(n), "i"(SLEEP) : "scc", "memory");
}

__device__ __forceinline__ void yield_until(unsigned addr, unsigned n) {
    unsigned v, st;
    asm volatile("ds_read_b32 %0, %2\n\ts_setprio 3\n\ts_waitcnt lgkmcnt(0)\n\tv_readfirstlane_b32 %1, %0\n\t"
                 "s_cmp_lt_u32 %1, %3\n\ts_cbranch_scc0 1f\n\ts_setprio 1\n1:"
                 : "=&v"(v), "=&s"(st) : "v"(addr), "s"(n) : "scc", "memory");
}

__device__ __forceinline__ void publish(char* ws, int off, u64 tag, int lane) {
    drain();
    if (lane == 0) __hip_atomic_store((GLBP u64*)(ws + off), tag, __ATOMIC_RELAXED, __HIP_MEMORY_SCOPE_AGENT);
}
template <int SLEEP>
__device__ __forceinline__ void wait_flags(char* ws, int off, bool active, u64 tag) {
#pragma unroll 1
    for (unsigned spin = 0; spin < SPIN_CAP; ++spin) {
        u64 v = tag;
        if (active) v = __hip_atomic_load((GLBP u64*)(ws + off), __ATOMIC_RELAXED, __HIP_MEMORY_SCOPE_AGENT);
        if (__all(v == tag)) break;
        __builtin_amdgcn_s_sleep(SLEEP);
    }
    asm volatile("" ::: "memory");
}

#define STR2(x) #x
#define STR(x) STR2(x)
#define EX(r) "v_exp_f32 v" STR(r) ", v" STR(r) "\n"
#define CV(d, a, b) "v_cvt_pk_bf16_f32 v" STR(d) ", v" STR(a) ", v" STR(b) "\n"
#define TILE(x0, x1, x2, x3, x4, x5, x6, x7, x8, x9, x10, x11, x12, x13, x14, x15, NEXT, u)     \
    "s_waitcnt lgkmcnt(2)\n"                                                                    \
    "v_mfma_f32_32x32x16_bf16 " NEXT ", v[104:107], %[q], v[116:131]\n"                         \
    "ds_read_b128 v[104:107], %[ka] offset:" STR((u + 2) * 1024) "\n"                           \
    EX(x0) EX(x1) EX(x2) EX(x3) CV(96, x0, x1) EX(x4) EX(x5) CV(97, x2, x3)                     \
    "s_waitcnt lgkmcnt(2)\n"                                                                    \
    "v_mfma_f32_32x32x16_bf16 %[o], v[112:115], v[100:103], %[o]\n"                             \
    "ds_read_b128 v[112:115], %[va] offset:" STR((2 * u + 1) * 512) "\n"                        \
    EX(x6) EX(x7) CV(98, x4, x5) EX(x8) CV(99, x6, x7) EX(x9) EX(x10) EX(x11)                   \
    "s_waitcnt lgkmcnt(2)\n"                                                                    \
    "v_mfma_f32_32x32x16_bf16 %[o], v[108:111], v[96:99], %[o]\n"                               \
    "ds_read_b128 v[108:111], %[va] offset:" STR((2 * u + 2) * 512) "\n"                        \
    EX(x12) EX(x13) CV(100, x8, x9) EX(x14) EX(x15) CV(101, x10, x11) CV(102, x12, x13)         \
    CV(103, x14, x15)
#define TILE_X(u) TILE(64, 65, 66, 67, 68, 69, 70, 71, 72, 73, 74, 75, 76, 77, 78, 79, "v[80:95]", u)
#define TILE_Y(u) TILE(80, 81, 82, 83, 84, 85, 86, 87, 88, 89, 90, 91, 92, 93, 94, 95, "v[64:79]", u)
#define MOVC(r) "v_mov_b32 v" STR(r) ", v116\n"
#define LDS_WAIT(off, n, lbl)                                                                   \
    lbl ":\n"                                                                                   \
    "ds_read_b32 v96, %[ca] offset:" STR(off) "\n"                                              \
    "s_waitcnt lgkmcnt(0)\n"                                                                    \
    "v_readfirstlane_b32 %[st], v96\n"                                                          \
    "s_cmp_lt_u32 %[st], " STR(n) "\n"                                                          \
    "s_cbranch_scc1 " lbl "b\n"
#define LDS_ARRIVE(off)                                                                         \
    "s_mov_b64 %[sx], exec\n"                                                                   \
    "s_mov_b64 exec, 1\n"                                                                       \
    "v_mov_b32 v96, 1\n"                                                                        \
    "ds_add_u32 %[ca], v96 offset:" STR(off) "\n"                                               \
    "s_mov_b64 exec, %[sx]\n"
#define ATT_CLOB                                                                                \
    "v64", "v65", "v66", "v67", "v68", "v69", "v70", "v71", "v72", "v73", "v74", "v75", "v76",  \
    "v77", "v78", "v79", "v80", "v81", "v82", "v83", "v84", "v85", "v86", "v87", "v88", "v89",  \
    "v90", "v91", "v92", "v93", "v94", "v95", "v96", "v97", "v98", "v99", "v100", "v101",       \
    "v102", "v103", "v104", "v105", "v106", "v107", "v108", "v109", "v110", "v111", "v112",     \
    "v113", "v114", "v115", "v116", "v117", "v118", "v119", "v120", "v121", "v122", "v123",     \
    "v124", "v125", "v126", "v127", "v128", "v129", "v130", "v131"

#define ITER_STAMP
#define ITER_OUT
#define ITER_CLOB
__device__ __forceinline__ f32x16 attn_pass(unsigned ka, unsigned va, unsigned vstep, unsigned ca,
                                            u32x4 q, float mgiven, int given) {
    f32x16 o = {0};
    int cnt, ev, st;
    u64 sx;
    asm volatile(
        "ds_read_b128 v[104:107], %[ka]\n"
        "v_mov_b32 v116, %[mg]\n"
        "s_cmp_lg_u32 %[gv], 0\n"
        "s_waitcnt lgkmcnt(0)\n"
        "s_cbranch_scc1 11f\n"
        "v_mfma_f32_32x32x16_bf16 v[64:79], v[104:107], %[q], 0\n"
        "s_nop 11\n"
        "v_max3_f32 v96, v64, v65, v66\n"
        "v_max3_f32 v97, v67, v68, v69\n"
        "v_max3_f32 v98, v70, v71, v72\n"
        "v_max3_f32 v99, v73, v74, v75\n"
        "v_max3_f32 v96, v96, v76, v77\n"
        "v_max3_f32 v97, v97, v78, v79\n"
        "v_max3_f32 v96, v96, v98, v99\n"
        "v_max_f32 v96, v96, v97\n"
        "v_mov_b32 v97, v96\n"
        "s_nop 1\n"
        "v_permlane32_swap_b32 v96, v97\n"
        "s_nop 1\n"
        "v_max_f32 v116, v96, v97\n"
        "11:\n"
        "v_xor_b32 v116, 0x80000000, v116\n"
        MOVC(117) MOVC(118) MOVC(119) MOVC(120) MOVC(121) MOVC(122) MOVC(123) MOVC(124)
        MOVC(125) MOVC(126) MOVC(127) MOVC(128) MOVC(129) MOVC(130) MOVC(131)
        "v_mov_b32 v100, 0\n v_mov_b32 v101, 0\n v_mov_b32 v102, 0\n v_mov_b32 v103, 0\n"
        "s_movk_i32 %[cnt], 16\n"
        "s_setprio 2\n"
        "v_mfma_f32_32x32x16_bf16 v[64:79], v[104:107], %[q], v[116:131]\n"
        "ds_read_b128 v[104:107], %[ka] offset:1024\n"
        "ds_read_b128 v[112:115], %[va]\n"
        "ds_read_b128 v[108:111], %[va]\n"
        "s_nop 10\n"
        "s_movk_i32 %[ev], 16\n"
        "12:\n"
        TILE_X(0) TILE_Y(1) TILE_X(2) TILE_Y(3)
        "v_add_u32 %[ka], 4096, %[ka]\n"
        "v_add_u32 %[va], %[vs], %[va]\n"
        ITER_STAMP
        "s_cmp_eq_u32 %[cnt], %[ev]\n"
        "s_cbranch_scc1 14f\n"
        "13:\n"
        "s_sub_u32 %[cnt], %[cnt], 1\n"
        "s_cmp_lg_u32 %[cnt], 0\n"
        "s_cbranch_scc1 12b\n"
        "s_branch 18f\n"
        "14:\n"
        "s_cmp_eq_u32 %[ev], 16\n"
        "s_cbranch_scc0 21f\n"
        LDS_WAIT(44, 2, "22")
        "s_movk_i32 %[ev], 14\n"
        "s_branch 13b\n"
        "21:\n"
        "s_cmp_eq_u32 %[ev], 14\n"
        "s_cbranch_scc0 15f\n"
        LDS_WAIT(4, 2, "17")
        "s_movk_i32 %[ev], 9\n"
        "s_branch 13b\n"
        "15:\n"
        "s_cmp_eq_u32 %[ev], 9\n"
        "s_cbranch_scc0 16f\n"
        LDS_WAIT(40, 2, "19")
        "s_setprio 1\n"
        "s_movk_i32 %[ev], 3\n"
        "s_branch 13b\n"
        "16:\n"
        "s_setprio 0\n"
        LDS_ARRIVE(8)
        "s_waitcnt lgkmcnt(0)\n"
        "s_branch 13b\n"
        "18:\n"
        "s_waitcnt lgkmcnt(0)\n"
        "v_mfma_f32_32x32x16_bf16 %[o], v[112:115], v[100:103], %[o]\n"
        "s_nop 15\n"
        : [o] "+v"(o), [ka] "+v"(ka), [va] "+v"(va), [cnt] "=&s"(cnt), [ev] "=&s"(ev), [st] "=&s"(st), [sx] "=&s"(sx) ITER_OUT
        : [q] "v"(q), [vs] "v"(vstep), [ca] "v"(ca), [mg] "v"(mgiven), [gv] "s"(given)
        : ATT_CLOB, "scc", "memory" ITER_CLOB);
    return o;
}

__device__ __forceinline__ float row_max(const char* kb, u32x4 q) {
    float m = -__builtin_inff();
    const f32x16 z = {0};
    for (int t = 0; t < 64; ++t) {
        const f32x16 s = mfma32(ld16(kb + t * 1024), q, z);
#pragma unroll
        for (int j = 0; j < 16; ++j) m = fmaxf(m, s[j]);
    }
    return fmaxf(m, __shfl_xor(m, 32));
}

#define GAP __builtin_amdgcn_sched_barrier(0); asm volatile("s_sleep 1"); __builtin_amdgcn_sched_barrier(0);
template <int T0, int T1, int T2, int T3>
__device__ __forceinline__ void conv_wave(__amdgpu_buffer_rsrc_t hr, const char* ul,
                                          __amdgpu_buffer_rsrc_t rs, int zoff, float dd, int lane) {
    constexpr int NF = T3 + 1;
    const int r = lane & 15, g = lane >> 4;
    const int ns = (SEQ - 1) - 16 * (g >> 1) - r + 8 * (g & 1);
    unsigned ap = (unsigned)(size_t)(LDSP const char*)ul + ((ns & 1) ? (U_RS + 2 * (ns - 1)) : 2 * ns) - 512 * (NF - 1);
    int b0 = 2 * (16 * r - 16 * (g >> 1) + 8 * (g & 1));
    f32x4 c0 = {0}, c1 = {0}, c2 = {0}, c3 = {0};
#pragma unroll 1
    for (int rho = 0; rho < 8; ++rho) {
        u32x4 A[NF], B[NF];
        int b1 = b0 + 512;
        asm volatile("" : "+v"(b1), "+v"(ap));
        B[0] = __builtin_amdgcn_raw_buffer_load_b128(hr, b0, 0, 0);
#pragma unroll
        for (int b = 1; b < NF; ++b) B[b] = __builtin_amdgcn_raw_buffer_load_b128(hr, b1 + 512 * (b - 1), 0, 0);
#pragma unroll
        for (int a = 0; a < NF; ++a) {
            volatile LDSP const unsigned* p = (volatile LDSP const unsigned*)(size_t)(ap + 512 * (NF - 1 - a));
            A[a] = (u32x4){p[0], p[1], p[2], p[3]};
        }
        __builtin_amdgcn_sched_barrier(0);
#pragma unroll
        for (int a = 0; a < NF; ++a) {
            c3 = mfma16(A[a], B[T3 - a], c3);
            GAP
            if (a <= T2) { c2 = mfma16(A[a], B[a <= T2 ? T2 - a : 0], c2); GAP }
            if (a <= T1) { c1 = mfma16(A[a], B[a <= T1 ? T1 - a : 0], c1); GAP }
            if (a <= T0) { c0 = mfma16(A[a], B[a <= T0 ? T0 - a : 0], c0); GAP }
        }
        b0 -= 64;
        ap -= 64;
    }
    const int ts[4] = {T0, T1, T2, T3};
    const f32x4 cs[4] = {c0, c1, c2, c3};
#pragma unroll
    for (int i = 0; i < 4; ++i) {
        const int s = 256 * ts[i] + 16 * r + 4 * g;
        const u32x2 uu = ld8(ul + 2 * ((SEQ - 4) - s));
        const u32x2 o = {pack2(rbf(cs[i][0]) + rbf(bfhi(uu[1]) * dd), rbf(cs[i][1]) + rbf(bflo(uu[1]) * dd)),
                         pack2(rbf(cs[i][2]) + rbf(bfhi(uu[0]) * dd), rbf(cs[i][3]) + rbf(bflo(uu[0]) * dd))};
        __builtin_amdgcn_raw_buffer_store_b64(o, rs, zoff + 2 * s, 0, 16);
    }
}

template <bool WFIRST>
__device__ __forceinline__ f32x4 kv_proj(const char* xb, const char* wp, int g, int c) {
    u32x4 fw[16], fx[16];
    f32x4 a = {0};
    asm volatile("" ::: "memory");
#define KV_LOAD(G)                                                                   \
    _Pragma("unroll") for (int s = 4 * G; s < 4 * G + 4; ++s) {                      \
        fw[s] = tr_frag(wp + s * 1024, 128);                                         \
        fx[s] = ld16(xb + G * 256 + (((4 * (s & 3) + g) ^ c) << 4));                 \
    }                                                                                \
    __builtin_amdgcn_sched_barrier(0);
#define KV_MMA(G)                                                                    \
    _Pragma("unroll") for (int s = 4 * G; s < 4 * G + 4; ++s)                        \
        a = WFIRST ? mfma16(fw[s], fx[s], a) : mfma16(fx[s], fw[s], a);              \
    __builtin_amdgcn_sched_barrier(0);
    KV_LOAD(0) KV_LOAD(1) KV_LOAD(2) KV_MMA(0) KV_LOAD(3) KV_MMA(1) KV_MMA(2) KV_MMA(3)
    return a;
}

__global__ void __launch_bounds__(768)
fused_kernel(const u16* __restrict__ x, const u16* __restrict__ wq, const u16* __restrict__ wk,
             const u16* __restrict__ wv, const u16* __restrict__ wo, const u16* __restrict__ bq,
             const u16* __restrict__ bk, const u16* __restrict__ bv, const u16* __restrict__ shk,
             const u16* __restrict__ ssmk, const u16* __restrict__ ssmd, u16* __restrict__ out,
             char* ws) {
    extern __shared__ __attribute__((aligned(16))) char smem[];
    const int lane = threadIdx.x & 63;
    const int w = __builtin_amdgcn_readfirstlane(threadIdx.x >> 6);
    const int blk = blockIdx.x;
    const int tg = (blk & 7) * 4 + (blk >> 6);
    const int hs = (blk >> 3) & 7;
    const int r = lane & 31, h = lane >> 5;
    const __amdgpu_buffer_rsrc_t rs = __builtin_amdgcn_make_buffer_rsrc(ws, 0, WS_END, 0x00020000);
    asm volatile("" ::"s"(x), "s"(wq), "s"(wk), "s"(wv), "s"(wo), "s"(bq), "s"(bk), "s"(bv), "s"(shk),
                 "s"(ssmk), "s"(ssmd), "s"(out), "s"(ws));

    const bool kvp = hs < 4;
    const int xt = hs & 1;
    const unsigned base = (unsigned)(size_t)(LDSP char*)smem;
    const unsigned cb = base + L_CNT;
    const int rot = 8 * (blk & 7);
    const u16* xsrc = x + (size_t)(64 * tg) * DM;
    const int us0 = 64 * tg + 16 * (w & 3) + 8 * h, ud0 = 64 * hs + 2 * r;
    unsigned kr[11], vr[8];
    u32x4 sk;
    if (w >= 8) {
        __builtin_amdgcn_s_setprio(3);
        const int wb4 = w - 8;
        if (kvp) {
#pragma unroll
            for (int j = 0; j < 8; ++j) {
                const int row = 32 * xt + 4 * j + wb4;
                dma16(xsrc + row * DM, (lane ^ (row & 15)) << 4, base + L_X + row * 1024);
            }
            const u16* src = (hs < 2 ? wk : wv) + wb4 * 2048;
#pragma unroll
            for (int j = 0; j < 4; ++j) dma16(src + j * 512, lane * 16, base + L_WKV + (wb4 * 4 + j) * 1024);
        }
        {
            const __amdgpu_buffer_rsrc_t kr_ = __builtin_amdgcn_make_buffer_rsrc((void*)bk, 0, SEQ * DM * 2, 0x00020000);
            const __amdgpu_buffer_rsrc_t vr_ = __builtin_amdgcn_make_buffer_rsrc((void*)bv, 0, SEQ * DM * 2, 0x00020000);
            const int o0 = (us0 * DM + ud0) * 2;
            int om = o0 - 3072;
            asm volatile("" : "+v"(om));
    #pragma unroll
            for (int j = 0; j < 3; ++j) kr[j] = __builtin_amdgcn_raw_buffer_load_b32(kr_, om + 1024 * j, 0, 0);
    #pragma unroll
            for (int j = 0; j < 8; ++j) {
                kr[3 + j] = __builtin_amdgcn_raw_buffer_load_b32(kr_, o0 + 1024 * (j & 3), 4096 * (j >> 2), 0);
                vr[j] = __builtin_amdgcn_raw_buffer_load_b32(vr_, o0 + 1024 * (j & 3), 4096 * (j >> 2), 0);
            }
            sk = ld16(shk + ud0 * 4);
        }
        if (wb4 == 0) {
            const int b3 = lane / 3, p = lane - 3 * b3;
            const unsigned v = (lane < 27 && p == 0) ? 0x3f803f80u : 0u;
            const unsigned dst = lane < 27 ? base + L_CONST + b3 * 512 + (p == 2 ? 64 : 16 * p) : cb + ((lane - 27) & 7) * 16;
            asm volatile("ds_write_b128 %0, %1" ::"v"(dst), "v"((u32x4){v, v, v, v}) : "memory");
        }
        barrier();
    } else {
        if (kvp) barrier();
        const u16* wsrc = wq + (size_t)(64 * w) * DM + 64 * hs;
        const unsigned wvo = (lane >> 3) * 1024 + (((lane & 7) ^ (((lane >> 4) & 1) << 2)) << 4);
#pragma unroll
        for (int j = 0; j < 4; ++j) {
            const int row = 32 * (xt ^ 1) + 8 * j + w;
            dma16(xsrc + row * DM, (lane ^ (row & 15)) << 4, base + L_X + row * 1024);
        }
        if (!kvp) {
#pragma unroll
            for (int j = 0; j < 4; ++j) {
                const int row = 32 * xt + 8 * j + w;
                dma16(xsrc + row * DM, (lane ^ (row & 15)) << 4, base + L_X + row * 1024);
            }
        }
#pragma unroll
        for (int j = 0; j < 8; ++j) dma16(wsrc + j * 8 * DM, wvo, base + L_W + (8 * w + j) * 1024);
    }
    u64 tag;
    asm volatile("s_load_dwordx2 %0, %1, 0x0\n\ts_waitcnt lgkmcnt(0)" : "=s"(tag) : "s"(ws) : "memory");
    tag += 1;

    const int tq = (lane & 15) >> 2, tp = lane & 3;
    if (w < 8) {
        if (!kvp) barrier();
        lds_arrive(cb + C_TAG);
        __builtin_amdgcn_s_setprio(2);
        const int i = w >> 2, hd = w & 3, kh = hd & 1;
        const int chunk = 4 * (hd >> 1) + 2 * ((lane >> 4) & 1) + (tp >> 1);
        const char* wb = smem + L_W + kh * 32768 + (8 * h + tq) * 128 + ((chunk ^ ((tq >> 1) << 2)) << 4) + 8 * (tp & 1);
        const char* xa[8];
#pragma unroll
        for (int s = 0; s < 8; ++s) {
            unsigned o = L_X + (32 * i + r) * 1024 + kh * 512 + (((2 * s + h) ^ (r & 15)) << 4);
            asm volatile("" : "+v"(o));
            xa[s] = smem + o;
        }
        drain();
        lds_arrive(cb + C_AIN);
        lds_wait<0>(cb + C_AIN, 8);
        if (kvp) lds_wait<0>(cb + C_KVRD, 2);
        f32x16 acc = {0};
        u32x4 fa[16], fb[16];
#define Q_LOAD(G)                                                                    \
    _Pragma("unroll") for (int s = 4 * G; s < 4 * G + 4; ++s) {                      \
        fa[s] = tr_frag(wb + s * 2048, 512);                                         \
        fb[s] = ld16(xa[s & 7] + (s >> 3) * 256);                                    \
    }                                                                                \
    __builtin_amdgcn_sched_barrier(0);
#define Q_MMA(G)                                                                     \
    _Pragma("unroll") for (int s = 4 * G; s < 4 * G + 4; ++s) acc = mfma32(fa[s], fb[s], acc); \
    __builtin_amdgcn_sched_barrier(0);
        const int goff = WS_KVG + ((w + rot) & 63) * 4096 + lane * 16;
        u32x4 gr[4];
        Q_LOAD(0) Q_LOAD(1) Q_MMA(0) Q_LOAD(2) Q_MMA(1) Q_LOAD(3)
#pragma unroll
        for (int j = 0; j < 4; ++j) gr[j] = __builtin_amdgcn_raw_buffer_load_b128(rs, goff + j * 1024, 0, 16);
        __builtin_amdgcn_sched_barrier(0);
        Q_MMA(2) Q_MMA(3)
        f32x4 keep[2];
        {
            f32x4* xp = reinterpret_cast<f32x4*>(smem + L_QX + (w ^ 1) * 2048) + lane;
#pragma unroll
            for (int g = 0; g < 2; ++g) {
                f32x4 lo = {acc[4 * g], acc[4 * g + 1], acc[4 * g + 2], acc[4 * g + 3]};
                f32x4 hi = {acc[8 + 4 * g], acc[9 + 4 * g], acc[10 + 4 * g], acc[11 + 4 * g]};
                xp[g * 64] = kh ? lo : hi;
                keep[g] = kh ? hi : lo;
            }
        }
        const unsigned ka = base + L_K + lane * 16;
        const unsigned va = base + (r < 16 ? L_V + h * 256 + r * 16
                                           : L_CONST + (r == 16 ? 0 : ((r >= 20 && r < 28) ? 64 : 16)));
        const unsigned vstep = r < 16 ? 4096 : 0;
        lds_arrive(cb + C_GATE);
        lds_wait<0>(cb + C_GATE, 8);
        const f32x4* xq = reinterpret_cast<const f32x4*>(smem + L_QX + w * 2048) + lane;
        const f32x4 qa = (xq[0] + keep[0]) * QSCALE, qb = (xq[64] + keep[1]) * QSCALE;
        const u32x4 q = {pack2(qa[0], qa[1]), pack2(qa[2], qa[3]), pack2(qb[0], qb[1]), pack2(qb[2], qb[3])};
#pragma unroll 1
        for (unsigned spin = 0; spin < SPIN_CAP; ++spin) {
            bool ok = true;
#pragma unroll
            for (int j = 0; j < 4; ++j) ok = ok && gr[j][1] == (unsigned)tag && gr[j][3] == (unsigned)tag;
            if (__all(ok)) break;
            __builtin_amdgcn_s_sleep(2);
#pragma unroll
            for (int j = 0; j < 4; ++j) gr[j] = __builtin_amdgcn_raw_buffer_load_b128(rs, goff + j * 1024, 0, 16);
        }
        *reinterpret_cast<u32x4*>(smem + L_K + w * 1024 + lane * 16) = (u32x4){gr[0][0], gr[0][2], gr[1][0], gr[1][2]};
        *reinterpret_cast<u32x4*>(smem + L_V + w * 1024 + lane * 16) = (u32x4){gr[2][0], gr[2][2], gr[3][0], gr[3][2]};
        lds_arrive(cb + C_FIRST);
        lds_wait<0>(cb + C_FIRST, 8);
        float m = 0.f;
        f32x16 o;
        float l;
#pragma unroll 1
        for (int given = 0; given < 2; ++given) {
            o = attn_pass(ka, va, vstep, cb, q, m, given);
            l = __shfl(o[8], r);
            float mag = 0.f;
#pragma unroll
            for (int j = 0; j < 8; ++j) mag += fabsf(o[j]);
            if (!__any(!(l < 1.0e18f) || !(mag < 3.0e38f))) break;
            m = row_max(smem + L_K + lane * 16, q);
        }
        const float inv = 1.0f / l;
        u32x4 of;
#pragma unroll
        for (int j = 0; j < 4; ++j) of[j] = pack2(o[2 * j] * inv, o[2 * j + 1] * inv);
        __builtin_amdgcn_raw_buffer_store_b128(
            of, rs, WS_OF + ((2 * tg + i) * 32 + 4 * hs + hd) * 1024 + lane * 16, 0, 16);
        publish(ws, WS_OFL + ((tg * 8 + hs) * 8 + w) * FL, tag, lane);
        return;
    }

    const int wb4 = w - 8;
    if (kvp) {
        asm volatile("s_waitcnt vmcnt(20)" ::: "memory");
        lds_arrive(cb + C_BIN);
        if (wb4 < 2) {
            const int tt = 2 * tg + xt, kind = hs >> 1;
            const int c = lane & 15, g = lane >> 4;
            const char* xb = smem + L_X + (32 * xt + 16 * wb4 + c) * 1024;
            const char* wp = smem + L_WKV + (8 * g + tq) * 32 + 8 * tp;
            const int pos = ((g & 1) ? (kind ? 256 : 512) : 0) + (wb4 ? (kind ? 512 : 256) : 0) + c * 16;
            lds_wait<0>(cb + C_BIN, 4);
            const f32x4 a = kind ? kv_proj<false>(xb, wp, g, c) : kv_proj<true>(xb, wp, g, c);
            const unsigned d0 = pack2(a[0], a[1]), d1 = pack2(a[2], a[3]);
            __builtin_amdgcn_raw_buffer_store_b128((u32x4){d0, (unsigned)tag, d1, (unsigned)tag}, rs,
                                                   WS_KVG + tt * 4096 + kind * 2048 + pos + (g >> 1) * 1024, 0, 16);
            __builtin_amdgcn_raw_buffer_store_b64((u32x2){d0, d1}, rs,
                                                  WS_KV + tt * 2048 + kind * 1024 + pos + (g >> 1) * 8, 0, 16);
            lds_arrive(cb + C_KVRD);
            drain();
            if (lds_arrive_count(cb + C_KVST) == 1 && lane < 8)
                __hip_atomic_store((GLBP u64*)(ws + WS_KVF + lane * KVF_REP + (tt * 2 + kind) * FL), tag,
                                   __ATOMIC_RELAXED, __HIP_MEMORY_SCOPE_AGENT);
        }
    }
    {
        __builtin_amdgcn_s_setprio(1);
#pragma unroll
        for (int e = 0; e < 2; ++e) {
            const float w0 = bflo(sk[2 * e]), w1 = bfhi(sk[2 * e]), w2 = bflo(sk[2 * e + 1]), w3 = bfhi(sk[2 * e + 1]);
            float kk[11], u[8];
#pragma unroll
            for (int j = 0; j < 11; ++j) kk[j] = e ? bfhi(kr[j]) : bflo(kr[j]);
#pragma unroll
            for (int j = 0; j < 8; j += 2) {
                const unsigned p3 = pack2(kk[j] * w3, kk[j + 1] * w3), p2 = pack2(kk[j + 1] * w2, kk[j + 2] * w2);
                const unsigned p1 = pack2(kk[j + 2] * w1, kk[j + 3] * w1), p0 = pack2(kk[j + 3] * w0, kk[j + 4] * w0);
                unsigned a = pack2(bflo(p3) + bflo(p2), bfhi(p3) + bfhi(p2));
                a = pack2(bflo(a) + bflo(p1), bfhi(a) + bfhi(p1));
                a = pack2(bflo(a) + bflo(p0), bfhi(a) + bfhi(p0));
                u[j] = bflo(a) * (e ? bfhi(vr[j]) : bflo(vr[j]));
                u[j + 1] = bfhi(a) * (e ? bfhi(vr[j + 1]) : bflo(vr[j + 1]));
            }
            const u32x4 o = {pack2(u[7], u[6]), pack2(u[5], u[4]), pack2(u[3], u[2]), pack2(u[1], u[0])};
            __builtin_amdgcn_raw_buffer_store_b128(o, rs, WS_UR + ((ud0 + e) * SEQ + (SEQ - 8) - us0) * 2, 0, 16);
            yield_until(cb + C_GATE, 8);
        }
        __builtin_amdgcn_s_setprio(3);
    }
    if (wb4 < 2) {
        lds_wait<1>(cb + C_GATE, 8);
        wait_flags<4>(ws, WS_KVF + (blk & 7) * KVF_REP + (wb4 * 64 + lane) * FL, true, tag);
        lds_arrive(cb + C_SEEN);
    }
    drain();
    if (lds_arrive_count(cb + C_UR) == 3) {
        lds_wait<1>(cb + C_TAG, 8);
        if (lane == 0)
            __hip_atomic_store((GLBP u64*)(ws + WS_URF + (hs * 32 + tg) * FL), tag, __ATOMIC_RELAXED, __HIP_MEMORY_SCOPE_AGENT);
    }
    {
        lds_wait<1>(cb + C_SEEN, 2);
        const int s0 = wb4 < 2 ? 8 : 40, ns = wb4 < 2 ? 32 : 24;
        const char* src = ws + WS_KV + (wb4 & 1) * 1024;
        const unsigned dst = base + ((wb4 & 1) ? L_V : L_K);
#pragma unroll 1
        for (int t = s0; t < s0 + ns; ++t) dma16_dev(src + ((t + rot) & 63) * 2048, lane * 16, dst + t * 1024);
        if (wb4 < 2) {
            asm volatile("s_waitcnt vmcnt(24)" ::: "memory");
            lds_arrive(cb + C_NEXT);
        }
        drain();
        lds_arrive(cb + (wb4 < 2 ? C_MID : C_LAST));
        lds_wait<1>(cb + C_FIRST, 8);
    }

    {
        const int ch = wb4 >> 1, sh = wb4 & 1;
        const int d = 2 * blk + ch;
        const float dd = bf2f(ssmd[d]);
        wait_flags<24>(ws, WS_URF + ((d >> 6) * 32 + lane) * FL, lane < 32, tag);
        char* ul = smem + L_U + ch * 2 * U_RS;
        u32x4 uv[4];
#pragma unroll
        for (int j = 0; j < 4; ++j)
            uv[j] = __builtin_amdgcn_raw_buffer_load_b128(rs, WS_UR + d * 4096 + j * 1024 + lane * 16 + 2 * sh, 0, 16);
        if (sh && lane == 63) uv[3][3] &= 0xffffu;
#pragma unroll
        for (int j = 0; j < 4; ++j)
            *reinterpret_cast<u32x4*>(ul + sh * U_RS + j * 1024 + lane * 16) = uv[j];
        if (lane < 2) *reinterpret_cast<u32x4*>(ul + sh * U_RS + 4096 + lane * 16) = (u32x4){0, 0, 0, 0};
        lds_arrive(cb + C_U + 4 * ch);
        lds_wait<1>(cb + C_U + 4 * ch, 2);
        const __amdgpu_buffer_rsrc_t hk = __builtin_amdgcn_make_buffer_rsrc((void*)(ssmk + (size_t)d * SEQ), 0, SEQ * 2, 0x00020000);
        if (sh) conv_wave<1, 2, 4, 7>(hk, ul, rs, WS_ZT + d * 4096, dd, lane);
        else conv_wave<0, 3, 5, 6>(hk, ul, rs, WS_ZT + d * 4096, dd, lane);
        publish(ws, WS_ZF + (d * 2 + sh) * FL, tag, lane);
    }

    const __amdgpu_buffer_rsrc_t ro = __builtin_amdgcn_make_buffer_rsrc(out, 0, SEQ * 1024 * 2, 0x00020000);
    if (wb4 < 2) {
        const int c8 = lane & 7;
        const int s0 = 64 * tg + 4 * ((lane >> 3) + 8 * wb4);
        const int d0 = 64 * hs + 8 * c8;
        u32x4 qr[4];
#pragma unroll
        for (int j = 0; j < 4; ++j) qr[j] = ld16(bq + (size_t)(s0 + j) * DM + d0);
        const int sh = (0x96 >> (tg >> 2)) & 1;
        wait_flags<24>(ws, WS_ZF + ((64 * hs + lane) * 2 + sh) * FL, true, tag);
        u32x2 z[8];
#pragma unroll
        for (int e = 0; e < 8; ++e)
            z[e] = __builtin_amdgcn_raw_buffer_load_b64(rs, WS_ZT + ((d0 + e) * SEQ + s0) * 2, 0, 16);
#pragma unroll
        for (int i = 0; i < 4; ++i) {
            u32x4 o;
#pragma unroll
            for (int j = 0; j < 4; ++j) {
                const unsigned za = z[2 * j][i >> 1], zb = z[2 * j + 1][i >> 1];
                const float a = (i & 1) ? bfhi(za) : bflo(za);
                const float b = (i & 1) ? bfhi(zb) : bflo(zb);
                o[j] = pack2(bflo(qr[i][j]) * a, bfhi(qr[i][j]) * b);
            }
            __builtin_amdgcn_raw_buffer_store_b128(o, ro, ((s0 + i) * 1024 + 512 + d0) * 2, 0, OUT_AUX);
        }
    } else if (blk == 0 && wb4 == 2) {
#pragma unroll 1
        for (int j = 0; j < 4; ++j) wait_flags<24>(ws, WS_URF + (j * 64 + lane) * FL, true, tag);
        if (lane == 0) __hip_atomic_store((GLBP u64*)(ws + WS_EPOCH), tag, __ATOMIC_RELAXED, __HIP_MEMORY_SCOPE_AGENT);
    }

    u32x4 fe[8], fo[8];
    {
        const __amdgpu_buffer_rsrc_t wr = __builtin_amdgcn_make_buffer_rsrc((void*)wo, 0, DM * DM * 2, 0x00020000);
        const int vo = (4 * h * DM + 2 * (r ^ (8 * wb4))) * 2;
        const int so = (128 * wb4 * DM + 64 * hs) * 2;
        unsigned wd[8][8];
#pragma unroll
        for (int s = 0; s < 8; ++s)
#pragma unroll
            for (int j = 0; j < 8; ++j)
                wd[s][j] = __builtin_amdgcn_raw_buffer_load_b32(wr, vo + (j & 3) * 1024, so + (16 * s + 8 * (j >> 2)) * 1024, 0);
        __builtin_amdgcn_sched_barrier(0);
#pragma unroll
        for (int s = 0; s < 8; ++s) {
#pragma unroll
            for (int j = 0; j < 4; ++j) {
                fe[s][j] = __builtin_amdgcn_perm(wd[s][2 * j + 1], wd[s][2 * j], 0x05040100u);
                fo[s][j] = __builtin_amdgcn_perm(wd[s][2 * j + 1], wd[s][2 * j], 0x07060302u);
            }
            asm volatile("" : "+v"(fe[s]), "+v"(fo[s]));
        }
    }
    lds_arrive(cb + C_TAIL);
    lds_wait<8>(cb + C_NEAR, 4);
#pragma unroll 1
    for (int i = 0; i < 2; ++i) {
        wait_flags<1>(ws, WS_OFL + ((tg * 8 + 2 * wb4 + (lane >> 2)) * 8 + 4 * i + (lane & 3)) * FL, lane < 8, tag);
        u32x4 of[8];
#pragma unroll
        for (int s = 0; s < 8; ++s)
            of[s] = __builtin_amdgcn_raw_buffer_load_b128(
                rs, lane * 16 + (s & 3) * 1024, WS_OF + ((2 * tg + i) * 32 + 8 * wb4 + (s & 4)) * 1024, 16);
        __builtin_amdgcn_sched_barrier(0);
        f32x16 ae = {0}, ao = {0};
#pragma unroll
        for (int s = 0; s < 8; ++s) {
            ae = mfma32(fe[s], of[s], ae);
            ao = mfma32(fo[s], of[s], ao);
        }
        lds_wait<0>(cb + C_TAIL, 4 + 8 * i);
#pragma unroll
        for (int g = 1; g < 4; ++g) {
            f32x4* p = reinterpret_cast<f32x4*>(smem + L_RED + ((g ^ wb4) * 3 + g - 1) * 2048) + lane;
            p[0] = (f32x4){ae[4 * g], ae[4 * g + 1], ae[4 * g + 2], ae[4 * g + 3]};
            p[64] = (f32x4){ao[4 * g], ao[4 * g + 1], ao[4 * g + 2], ao[4 * g + 3]};
        }
        lds_arrive(cb + C_TAIL);
        lds_wait<0>(cb + C_TAIL, 8 + 8 * i);
        f32x4 e = {ae[0], ae[1], ae[2], ae[3]}, o = {ao[0], ao[1], ao[2], ao[3]};
        const f32x4* p = reinterpret_cast<const f32x4*>(smem + L_RED + wb4 * 3 * 2048) + lane;
#pragma unroll
        for (int g = 0; g < 3; ++g) {
            e += p[g * 128];
            o += p[g * 128 + 64];
        }
        const u32x4 res = {pack2(e[0], o[0]), pack2(e[1], o[1]), pack2(e[2], o[2]), pack2(e[3], o[3])};
        __builtin_amdgcn_raw_buffer_store_b128(res, ro, ((64 * tg + 32 * i + r) * 1024 + 64 * hs + 16 * wb4 + 8 * h) * 2, 0, OUT_AUX);
        lds_arrive(cb + C_TAIL);
    }
}

extern "C" void kernel_launch(void* const* d_in, const int* in_sizes, int n_in, void* d_out,
                              int out_size, void* d_ws, size_t ws_size, hipStream_t stream) {
    (void)hipFuncSetAttribute((const void*)fused_kernel, hipFuncAttributeMaxDynamicSharedMemorySize, LDS_BYTES);
    fused_kernel<<<256, 768, LDS_BYTES, stream>>>(
        (const u16*)d_in[0], (const u16*)d_in[1], (const u16*)d_in[2], (const u16*)d_in[3],
        (const u16*)d_in[4], (const u16*)d_in[5], (const u16*)d_in[6], (const u16*)d_in[7],
        (const u16*)d_in[8], (const u16*)d_in[9], (const u16*)d_in[10], (u16*)d_out, (char*)d_ws);
}
```

```cpp
#include <hip/hip_runtime.h>
#include <hip/hip_bf16.h>
#define OUT_AUX 0
#define UWAIT 8
#define CSP 3

typedef __attribute__((ext_vector_type(8))) __bf16 bf16x8;
typedef __attribute__((ext_vector_type(2))) __bf16 bf16x2;
typedef __attribute__((ext_vector_type(16))) float f32x16;
typedef __attribute__((ext_vector_type(4))) float f32x4;
typedef __attribute__((ext_vector_type(2))) float f32x2;
typedef __attribute__((ext_vector_type(4))) unsigned u32x4;
typedef __attribute__((ext_vector_type(2))) unsigned u32x2;
typedef __attribute__((ext_vector_type(4))) short s16x4;
typedef unsigned long long u64;
typedef unsigned short u16;
#define LDSP __attribute__((address_space(3)))
#define GLBP __attribute__((address_space(1)))

constexpr int SEQ = 2048;
constexpr int DM = 512;

constexpr int FL = 128;
constexpr int WS_EPOCH = 0;
constexpr int KVF_REP = 1024 + 4096;
constexpr int WS_KVF = 4096;
constexpr int WS_URF = WS_KVF + 8 * KVF_REP;
constexpr int WS_ZF = WS_URF + 4096;
constexpr int WS_OFL = WS_ZF + 8192;
constexpr int WS_KV = WS_OFL + 2048 * FL;
constexpr int WS_KVG = WS_KV + 131072;
constexpr int WS_UR = WS_KVG + 262144;
constexpr int WS_ZT = WS_UR + 2097152 + 4096;
constexpr int WS_OF = WS_ZT + 2097152;
constexpr int WS_END = WS_OF + 2097152;

constexpr int L_X = 0;
constexpr int L_W = 65536;
constexpr int L_WKV = 147456;
constexpr int L_K = 0;
constexpr int L_V = 65536;
constexpr int L_CONST = 131072;
constexpr int L_CNT = L_CONST + 9 * 512;
constexpr int L_U = L_CNT + 128;
constexpr int U_RS = 4160;
constexpr int L_RED = L_U;
constexpr int L_QX = 147456;
constexpr int LDS_BYTES = 163840;
constexpr int C_FIRST = 0;
constexpr int C_MID = 4;
constexpr int C_NEAR = 8;
constexpr int C_BIN = 12;
constexpr int C_U = 16;
constexpr int C_KVRD = 24;
constexpr int C_Z = 64;
constexpr int C_TAG = 60;
constexpr int C_KVST = 56;
constexpr int C_AIN = 28;
constexpr int C_TAIL = 32;
constexpr int C_GATE = 36;
constexpr int C_LAST = 40;
constexpr int C_NEXT = 44;
constexpr int C_SEEN = 48;
constexpr int C_URS = 68;
constexpr int C_UR = 52;

constexpr float QSCALE = 0.25f * 1.4426950408889634f;
constexpr int CODE_BYTES = 12288;
constexpr unsigned SPIN_CAP = 1u << 18;

__device__ __forceinline__ float bf2f(u16 v) { return __uint_as_float(((unsigned)v) << 16); }
__device__ __forceinline__ float bflo(unsigned v) { return __uint_as_float(v << 16); }
__device__ __forceinline__ float bfhi(unsigned v) { return __uint_as_float(v & 0xffff0000u); }
__device__ __forceinline__ unsigned pack2(float lo, float hi) {
    f32x2 f = {lo, hi};
    bf16x2 b = __builtin_convertvector(f, bf16x2);
    return __builtin_bit_cast(unsigned, b);
}
__device__ __forceinline__ float rbf(float x) { return (float)(__bf16)x; }
__device__ __forceinline__ bf16x8 as_bf(u32x4 v) { return __builtin_bit_cast(bf16x8, v); }
__device__ __forceinline__ f32x16 mfma32(u32x4 a, u32x4 b, f32x16 c) {
    return __builtin_amdgcn_mfma_f32_32x32x16_bf16(as_bf(a), as_bf(b), c, 0, 0, 0);
}
__device__ __forceinline__ f32x4 mfma16(u32x4 a, u32x4 b, f32x4 c) {
    return __builtin_amdgcn_mfma_f32_16x16x32_bf16(as_bf(a), as_bf(b), c, 0, 0, 0);
}
__device__ __forceinline__ u32x4 ld16(const void* p) { return *reinterpret_cast<const u32x4*>(p); }
__device__ __forceinline__ u32x2 ld8(const void* p) { return *reinterpret_cast<const u32x2*>(p); }
__device__ __forceinline__ u32x4 tr_frag(const char* p, int second) {
    const s16x4 a = __builtin_amdgcn_ds_read_tr16_b64_v4i16((LDSP s16x4*)p);
    const s16x4 b = __builtin_amdgcn_ds_read_tr16_b64_v4i16((LDSP s16x4*)(p + second));
    const u32x2 a2 = __builtin_bit_cast(u32x2, a), b2 = __builtin_bit_cast(u32x2, b);
    return (u32x4){a2[0], a2[1], b2[0], b2[1]};
}
__device__ __forceinline__ void dma16(const void* src, unsigned voff, unsigned dst) {
    asm volatile("s_mov_b32 m0, %2\n\ts_nop 0\n\tglobal_load_lds_dwordx4 %0, %1" ::"v"(voff), "s"(src), "s"(dst) : "memory");
}
__device__ __forceinline__ void dma16_dev(const void* src, unsigned voff, unsigned dst) {
    asm volatile("s_mov_b32 m0, %2\n\ts_nop 0\n\tglobal_load_lds_dwordx4 %0, %1 sc1" ::"v"(voff), "s"(src), "s"(dst) : "memory");
}
__device__ __forceinline__ void barrier() {
    asm volatile("s_waitcnt lgkmcnt(0)\n\ts_barrier" ::: "memory");
}
__device__ __forceinline__ void drain() { asm volatile("s_waitcnt vmcnt(0)" ::: "memory"); }

__device__ __forceinline__ void lds_arrive(unsigned addr) {
    u64 sx;
    asm volatile("s_waitcnt lgkmcnt(0)\n\ts_mov_b64 %0, exec\n\ts_mov_b64 exec, 1\n\t"
                 "ds_add_u32 %1, %2\n\ts_mov_b64 exec, %0"
                 : "=&s"(sx) : "v"(addr), "v"(1u) : "memory");
}
__device__ __forceinline__ unsigned lds_arrive_count(unsigned addr) {
    u64 sx;
    unsigned v, st;
    asm volatile("s_waitcnt lgkmcnt(0)\n\ts_mov_b64 %0, exec\n\ts_mov_b64 exec, 1\n\t"
                 "ds_add_rtn_u32 %1, %3, %4\n\ts_waitcnt lgkmcnt(0)\n\tv_readfirstlane_b32 %2, %1\n\ts_mov_b64 exec, %0"
                 : "=&s"(sx), "=&v"(v), "=&s"(st) : "v"(addr), "v"(1u) : "memory");
    return st;
}
template <int SLEEP>
__device__ __forceinline__ void lds_wait(unsigned addr, unsigned n) {
    unsigned v, st;
    asm volatile("s_branch 2f\n1:\n\ts_sleep %4\n2:\n\tds_read_b32 %0, %2\n\ts_waitcnt lgkmcnt(0)\n\t"
                 "v_readfirstlane_b32 %1, %0\n\ts_cmp_lt_u32 %1, %3\n\ts_cbranch_scc1 1b"
                 : "=&v"(v), "=&s"(st) : "v"(addr), "s"(n), "i"(SLEEP) : "scc", "memory");
}

__device__ __forceinline__ void publish(char* ws, int off, u64 tag, int lane) {
    drain();
    if (lane == 0) __hip_atomic_store((GLBP u64*)(ws + off), tag, __ATOMIC_RELAXED, __HIP_MEMORY_SCOPE_AGENT);
}
template <int SLEEP>
__device__ __forceinline__ void wait_flags(char* ws, int off, bool active, u64 tag) {
#pragma unroll 1
    for (unsigned spin = 0; spin < SPIN_CAP; ++spin) {
        u64 v = tag;
        if (active) v = __hip_atomic_load((GLBP u64*)(ws + off), __ATOMIC_RELAXED, __HIP_MEMORY_SCOPE_AGENT);
        if (__all(v == tag)) break;
        __builtin_amdgcn_s_sleep(SLEEP);
    }
    asm volatile("" ::: "memory");
}

#define STR2(x) #x
#define STR(x) STR2(x)
#define EX(r) "v_exp_f32 v" STR(r) ", v" STR(r) "\n"
#define CV(d, a, b) "v_cvt_pk_bf16_f32 v" STR(d) ", v" STR(a) ", v" STR(b) "\n"
#define TILE(x0, x1, x2, x3, x4, x5, x6, x7, x8, x9, x10, x11, x12, x13, x14, x15, NEXT, u)     \
    "s_waitcnt lgkmcnt(2)\n"                                                                    \
    "v_mfma_f32_32x32x16_bf16 " NEXT ", v[104:107], %[q], v[116:131]\n"                         \
    "ds_read_b128 v[104:107], %[ka] offset:" STR((u + 2) * 1024) "\n"                           \
    EX(x0) EX(x1) EX(x2) EX(x3) CV(96, x0, x1) EX(x4) EX(x5) CV(97, x2, x3)                     \
    "s_waitcnt lgkmcnt(2)\n"                                                                    \
    "v_mfma_f32_32x32x16_bf16 %[o], v[112:115], v[100:103], %[o]\n"                             \
    "ds_read_b128 v[112:115], %[va] offset:" STR((2 * u + 1) * 512) "\n"                        \
    EX(x6) EX(x7) CV(98, x4, x5) EX(x8) CV(99, x6, x7) EX(x9) EX(x10) EX(x11)                   \
    "s_waitcnt lgkmcnt(2)\n"                                                                    \
    "v_mfma_f32_32x32x16_bf16 %[o], v[108:111], v[96:99], %[o]\n"                               \
    "ds_read_b128 v[108:111], %[va] offset:" STR((2 * u + 2) * 512) "\n"                        \
    EX(x12) EX(x13) CV(100, x8, x9) EX(x14) EX(x15) CV(101, x10, x11) CV(102, x12, x13)         \
    CV(103, x14, x15)
#define TILE_X(u) TILE(64, 65, 66, 67, 68, 69, 70, 71, 72, 73, 74, 75, 76, 77, 78, 79, "v[80:95]", u)
#define TILE_Y(u) TILE(80, 81, 82, 83, 84, 85, 86, 87, 88, 89, 90, 91, 92, 93, 94, 95, "v[64:79]", u)
#define MOVC(r) "v_mov_b32 v" STR(r) ", v116\n"
#define LDS_WAIT(off, n, lbl)                                                                   \
    lbl ":\n"                                                                                   \
    "ds_read_b32 v96, %[ca] offset:" STR(off) "\n"                                              \
    "s_waitcnt lgkmcnt(0)\n"                                                                    \
    "v_readfirstlane_b32 %[st], v96\n"                                                          \
    "s_cmp_lt_u32 %[st], " STR(n) "\n"                                                          \
    "s_cbranch_scc1 " lbl "b\n"
#define LDS_ARRIVE(off)                                                                         \
    "s_mov_b64 %[sx], exec\n"                                                                   \
    "s_mov_b64 exec, 1\n"                                                                       \
    "v_mov_b32 v96, 1\n"                                                                        \
    "ds_add_u32 %[ca], v96 offset:" STR(off) "\n"                                               \
    "s_mov_b64 exec, %[sx]\n"
#define ATT_CLOB                                                                                \
    "v64", "v65", "v66", "v67", "v68", "v69", "v70", "v71", "v72", "v73", "v74", "v75", "v76",  \
    "v77", "v78", "v79", "v80", "v81", "v82", "v83", "v84", "v85", "v86", "v87", "v88", "v89",  \
    "v90", "v91", "v92", "v93", "v94", "v95", "v96", "v97", "v98", "v99", "v100", "v101",       \
    "v102", "v103", "v104", "v105", "v106", "v107", "v108", "v109", "v110", "v111", "v112",     \
    "v113", "v114", "v115", "v116", "v117", "v118", "v119", "v120", "v121", "v122", "v123",     \
    "v124", "v125", "v126", "v127", "v128", "v129", "v130", "v131"

#define ITER_STAMP
#define ITER_OUT
#define ITER_CLOB
__device__ __forceinline__ f32x16 attn_pass(unsigned ka, unsigned va, unsigned vstep, unsigned ca,
                                            u32x4 q, float mgiven, int given) {
    f32x16 o = {0};
    int cnt, ev, st;
    u64 sx;
    asm volatile(
        "ds_read_b128 v[104:107], %[ka]\n"
        "v_mov_b32 v116, %[mg]\n"
        "s_cmp_lg_u32 %[gv], 0\n"
        "s_waitcnt lgkmcnt(0)\n"
        "s_cbranch_scc1 11f\n"
        "v_mfma_f32_32x32x16_bf16 v[64:79], v[104:107], %[q], 0\n"
        "s_nop 11\n"
        "v_max3_f32 v96, v64, v65, v66\n"
        "v_max3_f32 v97, v67, v68, v69\n"
        "v_max3_f32 v98, v70, v71, v72\n"
        "v_max3_f32 v99, v73, v74, v75\n"
        "v_max3_f32 v96, v96, v76, v77\n"
        "v_max3_f32 v97, v97, v78, v79\n"
        "v_max3_f32 v96, v96, v98, v99\n"
        "v_max_f32 v96, v96, v97\n"
        "v_mov_b32 v97, v96\n"
        "s_nop 1\n"
        "v_permlane32_swap_b32 v96, v97\n"
        "s_nop 1\n"
        "v_max_f32 v116, v96, v97\n"
        "11:\n"
        "v_xor_b32 v116, 0x80000000, v116\n"
        MOVC(117) MOVC(118) MOVC(119) MOVC(120) MOVC(121) MOVC(122) MOVC(123) MOVC(124)
        MOVC(125) MOVC(126) MOVC(127) MOVC(128) MOVC(129) MOVC(130) MOVC(131)
        "v_mov_b32 v100, 0\n v_mov_b32 v101, 0\n v_mov_b32 v102, 0\n v_mov_b32 v103, 0\n"
        "s_movk_i32 %[cnt], 16\n"
        "s_setprio 2\n"
        "v_mfma_f32_32x32x16_bf16 v[64:79], v[104:107], %[q], v[116:131]\n"
        "ds_read_b128 v[104:107], %[ka] offset:1024\n"
        "ds_read_b128 v[112:115], %[va]\n"
        "ds_read_b128 v[108:111], %[va]\n"
        "s_nop 10\n"
        "s_movk_i32 %[ev], 16\n"
        "12:\n"
        TILE_X(0) TILE_Y(1) TILE_X(2) TILE_Y(3)
        "v_add_u32 %[ka], 4096, %[ka]\n"
        "v_add_u32 %[va], %[vs], %[va]\n"
        ITER_STAMP
        "s_cmp_eq_u32 %[cnt], %[ev]\n"
        "s_cbranch_scc1 14f\n"
        "13:\n"
        "s_sub_u32 %[cnt], %[cnt], 1\n"
        "s_cmp_lg_u32 %[cnt], 0\n"
        "s_cbranch_scc1 12b\n"
        "s_branch 18f\n"
        "14:\n"
        "s_cmp_eq_u32 %[ev], 16\n"
        "s_cbranch_scc0 21f\n"
        LDS_WAIT(44, 2, "22")
        "s_movk_i32 %[ev], 14\n"
        "s_branch 13b\n"
        "21:\n"
        "s_cmp_eq_u32 %[ev], 14\n"
        "s_cbranch_scc0 15f\n"
        LDS_WAIT(4, 2, "17")
        "s_movk_i32 %[ev], 9\n"
        "s_branch 13b\n"
        "15:\n"
        "s_cmp_eq_u32 %[ev], 9\n"
        "s_cbranch_scc0 16f\n"
        LDS_WAIT(40, 2, "19")
        "s_setprio 1\n"
        "s_movk_i32 %[ev], 3\n"
        "s_branch 13b\n"
        "16:\n"
        "s_setprio 0\n"
        LDS_ARRIVE(8)
        "s_waitcnt lgkmcnt(0)\n"
        "s_branch 13b\n"
        "18:\n"
        "s_waitcnt lgkmcnt(0)\n"
        "v_mfma_f32_32x32x16_bf16 %[o], v[112:115], v[100:103], %[o]\n"
        "s_nop 15\n"
        : [o] "+v"(o), [ka] "+v"(ka), [va] "+v"(va), [cnt] "=&s"(cnt), [ev] "=&s"(ev), [st] "=&s"(st), [sx] "=&s"(sx) ITER_OUT
        : [q] "v"(q), [vs] "v"(vstep), [ca] "v"(ca), [mg] "v"(mgiven), [gv] "s"(given)
        : ATT_CLOB, "scc", "memory" ITER_CLOB);
    return o;
}

__device__ __forceinline__ float row_max(const char* kb, u32x4 q) {
    float m = -__builtin_inff();
    const f32x16 z = {0};
    for (int t = 0; t < 64; ++t) {
        const f32x16 s = mfma32(ld16(kb + t * 1024), q, z);
#pragma unroll
        for (int j = 0; j < 16; ++j) m = fmaxf(m, s[j]);
    }
    return fmaxf(m, __shfl_xor(m, 32));
}

#define GAP __builtin_amdgcn_sched_barrier(0); asm volatile("s_sleep 1"); __builtin_amdgcn_sched_barrier(0);
template <int T0, int T1, int T2, int T3>
__device__ __forceinline__ void conv_wave(__amdgpu_buffer_rsrc_t hr, const char* ul,
                                          __amdgpu_buffer_rsrc_t rs, int zoff, float dd, int lane) {
    constexpr int NF = T3 + 1;
    const int r = lane & 15, g = lane >> 4;
    const int ns = (SEQ - 1) - 16 * (g >> 1) - r + 8 * (g & 1);
    unsigned ap = (unsigned)(size_t)(LDSP const char*)ul + ((ns & 1) ? (U_RS + 2 * (ns - 1)) : 2 * ns) - 512 * (NF - 1);
    int b0 = 2 * (16 * r - 16 * (g >> 1) + 8 * (g & 1));
    f32x4 c0 = {0}, c1 = {0}, c2 = {0}, c3 = {0};
#pragma unroll 1
    for (int rho = 0; rho < 8; ++rho) {
        u32x4 A[NF], B[NF];
        int b1 = b0 + 512;
        asm volatile("" : "+v"(b1), "+v"(ap));
        B[0] = __builtin_amdgcn_raw_buffer_load_b128(hr, b0, 0, 0);
#pragma unroll
        for (int b = 1; b < NF; ++b) B[b] = __builtin_amdgcn_raw_buffer_load_b128(hr, b1 + 512 * (b - 1), 0, 0);
#pragma unroll
        for (int a = 0; a < NF; ++a) {
            volatile LDSP const unsigned* p = (volatile LDSP const unsigned*)(size_t)(ap + 512 * (NF - 1 - a));
            A[a] = (u32x4){p[0], p[1], p[2], p[3]};
        }
        __builtin_amdgcn_sched_barrier(0);
#pragma unroll
        for (int a = 0; a < NF; ++a) {
            c3 = mfma16(A[a], B[T3 - a], c3);
            GAP
            if (a <= T2) { c2 = mfma16(A[a], B[a <= T2 ? T2 - a : 0], c2); GAP }
            if (a <= T1) { c1 = mfma16(A[a], B[a <= T1 ? T1 - a : 0], c1); GAP }
            if (a <= T0) { c0 = mfma16(A[a], B[a <= T0 ? T0 - a : 0], c0); GAP }
        }
        b0 -= 64;
        ap -= 64;
    }
    const int ts[4] = {T0, T1, T2, T3};
    const f32x4 cs[4] = {c0, c1, c2, c3};
#pragma unroll
    for (int i = 0; i < 4; ++i) {
        const int s = 256 * ts[i] + 16 * r + 4 * g;
        const u32x2 uu = ld8(ul + 2 * ((SEQ - 4) - s));
        const u32x2 o = {pack2(rbf(cs[i][0]) + rbf(bfhi(uu[1]) * dd), rbf(cs[i][1]) + rbf(bflo(uu[1]) * dd)),
                         pack2(rbf(cs[i][2]) + rbf(bfhi(uu[0]) * dd), rbf(cs[i][3]) + rbf(bflo(uu[0]) * dd))};
        __builtin_amdgcn_raw_buffer_store_b64(o, rs, zoff + 2 * s, 0, 16);
    }
}

template <bool WFIRST>
__device__ __forceinline__ f32x4 kv_proj(const char* xb, const char* wp, int g, int c) {
    u32x4 fw[16], fx[16];
    f32x4 a = {0};
    asm volatile("" ::: "memory");
#define KV_LOAD(G)                                                                   \
    _Pragma("unroll") for (int s = 4 * G; s < 4 * G + 4; ++s) {                      \
        fw[s] = tr_frag(wp + s * 1024, 128);                                         \
        fx[s] = ld16(xb + G * 256 + (((4 * (s & 3) + g) ^ c) << 4));                 \
    }                                                                                \
    __builtin_amdgcn_sched_barrier(0);
#define KV_MMA(G)                                                                    \
    _Pragma("unroll") for (int s = 4 * G; s < 4 * G + 4; ++s)                        \
        a = WFIRST ? mfma16(fw[s], fx[s], a) : mfma16(fx[s], fw[s], a);              \
    __builtin_amdgcn_sched_barrier(0);
    KV_LOAD(0) KV_LOAD(1) KV_LOAD(2) KV_MMA(0) KV_LOAD(3) KV_MMA(1) KV_MMA(2) KV_MMA(3)
    return a;
}

__global__ void __launch_bounds__(768)
fused_kernel(const u16* __restrict__ x, const u16* __restrict__ wq, const u16* __restrict__ wk,
             const u16* __restrict__ wv, const u16* __restrict__ wo, const u16* __restrict__ bq,
             const u16* __restrict__ bk, const u16* __restrict__ bv, const u16* __restrict__ shk,
             const u16* __restrict__ ssmk, const u16* __restrict__ ssmd, u16* __restrict__ out,
             char* ws) {
    extern __shared__ __attribute__((aligned(16))) char smem[];
    const int lane = threadIdx.x & 63;
    const int w = __builtin_amdgcn_readfirstlane(threadIdx.x >> 6);
    const int blk = blockIdx.x;
    const int tg = (blk & 7) * 4 + (blk >> 6);
    const int hs = (blk >> 3) & 7;
    const int r = lane & 31, h = lane >> 5;
    const __amdgpu_buffer_rsrc_t rs = __builtin_amdgcn_make_buffer_rsrc(ws, 0, WS_END, 0x00020000);
    asm volatile("" ::"s"(x), "s"(wq), "s"(wk), "s"(wv), "s"(wo), "s"(bq), "s"(bk), "s"(bv), "s"(shk),
                 "s"(ssmk), "s"(ssmd), "s"(out), "s"(ws));
    const __amdgpu_buffer_rsrc_t ro = __builtin_amdgcn_make_buffer_rsrc(out, 0, SEQ * 1024 * 2, 0x00020000);

    const bool kvp = hs < 4;
    const int xt = hs & 1;
    const unsigned base = (unsigned)(size_t)(LDSP char*)smem;
    const unsigned cb = base + L_CNT;
    const int rot = 8 * (blk & 7);
    const u16* xsrc = x + (size_t)(64 * tg) * DM;
    const int us0 = 64 * tg + 8 * (lane >> 3), ud0 = 64 * hs + 16 * (w & 3) + 2 * (lane & 7);
    unsigned kr[11], vr[8];
    u32x4 sk;
    if (w >= 8) {
        __builtin_amdgcn_s_setprio(3);
        const int wb4 = w - 8;
        if (kvp) {
#pragma unroll 1
            for (int row = 32 * xt + wb4; row < 32 * xt + 32; row += 4)
                dma16(xsrc + row * DM, (lane ^ (row & 15)) << 4, base + L_X + row * 1024);
            const u16* src = hs < 2 ? wk : wv;
#pragma unroll 1
            for (int j = wb4; j < 16; j += 4) dma16(src + j * 512, lane * 16, base + L_WKV + j * 1024);
        }
        if (wb4 == 0) {
            const int b3 = lane / 3, p = lane - 3 * b3;
            const unsigned v = (lane < 27 && p == 0) ? 0x3f803f80u : 0u;
            const unsigned dst = lane < 27 ? base + L_CONST + b3 * 512 + (p == 2 ? 64 : 16 * p) : cb + ((lane - 27) & 7) * 16;
            asm volatile("ds_write_b128 %0, %1" ::"v"(dst), "v"((u32x4){v, v, v, v}) : "memory");
        }
        barrier();
        if (hs == 7 && wb4 == 3) {
            const char* pc;
            asm volatile("s_getpc_b64 %0" : "=s"(pc));
            const GLBP unsigned* cp = (const GLBP unsigned*)(pc + lane * 64);
            unsigned sink = 0;
#pragma unroll
            for (int j = 0; j < CODE_BYTES / 4096; ++j) sink += cp[j * 1024];
            asm volatile("" ::"v"(sink));
        }
    } else {
        barrier();
        const u16* wsrc = wq + (size_t)(64 * w) * DM + 64 * hs;
        const unsigned wvo = (lane >> 3) * 1024 + (((lane & 7) ^ (((lane >> 4) & 1) << 2)) << 4);
        const int r0 = kvp ? 32 * (xt ^ 1) : 0, r1 = kvp ? r0 + 32 : 64;
#pragma unroll 1
        for (int row = r0 + w; row < r1; row += 8)
            dma16(xsrc + row * DM, (lane ^ (row & 15)) << 4, base + L_X + row * 1024);
#pragma unroll 1
        for (int j = 0; j < 8; ++j) dma16(wsrc + j * 8 * DM, wvo, base + L_W + (8 * w + j) * 1024);
    }
    u64 tag;
    asm volatile("s_load_dwordx2 %0, %1, 0x0\n\ts_waitcnt lgkmcnt(0)" : "=s"(tag) : "s"(ws) : "memory");
    tag += 1;

    const int tq = (lane & 15) >> 2, tp = lane & 3;
    if (w < 8) {
        lds_arrive(cb + C_TAG);
        __builtin_amdgcn_s_setprio(2);
        const int i = w >> 2, hd = w & 3, kh = hd & 1;
        const int chunk = 4 * (hd >> 1) + 2 * ((lane >> 4) & 1) + (tp >> 1);
        const char* wb = smem + L_W + kh * 32768 + (8 * h + tq) * 128 + ((chunk ^ ((tq >> 1) << 2)) << 4) + 8 * (tp & 1);
        const char* xa[8];
#pragma unroll
        for (int s = 0; s < 8; ++s) {
            unsigned o = L_X + (32 * i + r) * 1024 + kh * 512 + (((2 * s + h) ^ (r & 15)) << 4);
            asm volatile("" : "+v"(o));
            xa[s] = smem + o;
        }
        drain();
        lds_arrive(cb + C_AIN);
        lds_wait<0>(cb + C_AIN, 8);
        if (kvp) lds_wait<0>(cb + C_KVRD, 2);
        f32x16 acc = {0};
        u32x4 fa[16], fb[16];
#define Q_LOAD(G)                                                                    \
    _Pragma("unroll") for (int s = 4 * G; s < 4 * G + 4; ++s) {                      \
        fa[s] = tr_frag(wb + s * 2048, 512);                                         \
        fb[s] = ld16(xa[s & 7] + (s >> 3) * 256);                                    \
    }                                                                                \
    __builtin_amdgcn_sched_barrier(0);
#define Q_MMA(G)                                                                     \
    _Pragma("unroll") for (int s = 4 * G; s < 4 * G + 4; ++s) acc = mfma32(fa[s], fb[s], acc); \
    __builtin_amdgcn_sched_barrier(0);
        const int goff = WS_KVG + ((w + rot) & 63) * 4096 + lane * 16;
        u32x4 gr[4];
        Q_LOAD(0) Q_LOAD(1) Q_MMA(0) Q_LOAD(2) Q_MMA(1) Q_LOAD(3)
#pragma unroll
        for (int j = 0; j < 4; ++j) gr[j] = __builtin_amdgcn_raw_buffer_load_b128(rs, goff + j * 1024, 0, 16);
        __builtin_amdgcn_sched_barrier(0);
        Q_MMA(2) Q_MMA(3)
        f32x4 keep[2];
        {
            f32x4* xp = reinterpret_cast<f32x4*>(smem + L_QX + (w ^ 1) * 2048) + lane;
#pragma unroll
            for (int g = 0; g < 2; ++g) {
                f32x4 lo = {acc[4 * g], acc[4 * g + 1], acc[4 * g + 2], acc[4 * g + 3]};
                f32x4 hi = {acc[8 + 4 * g], acc[9 + 4 * g], acc[10 + 4 * g], acc[11 + 4 * g]};
                xp[g * 64] = kh ? lo : hi;
                keep[g] = kh ? hi : lo;
            }
        }
        const unsigned ka = base + L_K + lane * 16;
        const unsigned va = base + (r < 16 ? L_V + h * 256 + r * 16
                                           : L_CONST + (r == 16 ? 0 : ((r >= 20 && r < 28) ? 64 : 16)));
        const unsigned vstep = r < 16 ? 4096 : 0;
        lds_arrive(cb + C_GATE);
#pragma unroll 1
        for (unsigned spin = 0; spin < SPIN_CAP; ++spin) {
            bool ok = true;
#pragma unroll
            for (int j = 0; j < 4; ++j) ok = ok && gr[j][1] == (unsigned)tag && gr[j][3] == (unsigned)tag;
            if (__all(ok)) break;
            __builtin_amdgcn_s_sleep(2);
#pragma unroll
            for (int j = 0; j < 4; ++j) gr[j] = __builtin_amdgcn_raw_buffer_load_b128(rs, goff + j * 1024, 0, 16);
        }
        if (w == 0) {
#pragma unroll 1
            for (unsigned spin = 0; spin < SPIN_CAP; ++spin) {
                const u32x4 f = __builtin_amdgcn_raw_buffer_load_b128(rs, WS_KVF + (blk & 7) * KVF_REP + lane * 16, 0, 16);
                const unsigned lo = (unsigned)tag, hi = (unsigned)(tag >> 32);
                if (__all(f[0] == lo && f[1] == hi && f[2] == lo && f[3] == hi)) break;
                __builtin_amdgcn_s_sleep(6);
            }
            lds_arrive(cb + C_SEEN);
        }
        lds_wait<0>(cb + C_GATE, 8);
        const f32x4* xq = reinterpret_cast<const f32x4*>(smem + L_QX + w * 2048) + lane;
        const f32x4 qa = (xq[0] + keep[0]) * QSCALE, qb = (xq[64] + keep[1]) * QSCALE;
        const u32x4 q = {pack2(qa[0], qa[1]), pack2(qa[2], qa[3]), pack2(qb[0], qb[1]), pack2(qb[2], qb[3])};
        *reinterpret_cast<u32x4*>(smem + L_K + w * 1024 + lane * 16) = (u32x4){gr[0][0], gr[0][2], gr[1][0], gr[1][2]};
        *reinterpret_cast<u32x4*>(smem + L_V + w * 1024 + lane * 16) = (u32x4){gr[2][0], gr[2][2], gr[3][0], gr[3][2]};
        lds_arrive(cb + C_FIRST);
        lds_wait<0>(cb + C_FIRST, 8);
        float m = 0.f;
        f32x16 o;
        float l;
#pragma unroll 1
        for (int given = 0; given < 2; ++given) {
            o = attn_pass(ka, va, vstep, cb, q, m, given);
            l = __shfl(o[8], r);
            float mag = 0.f;
#pragma unroll
            for (int j = 0; j < 8; ++j) mag += fabsf(o[j]);
            if (!__any(!(l < 1.0e18f) || !(mag < 3.0e38f))) break;
            m = row_max(smem + L_K + lane * 16, q);
        }
        const float inv = 1.0f / l;
        u32x4 of;
#pragma unroll
        for (int j = 0; j < 4; ++j) of[j] = pack2(o[2 * j] * inv, o[2 * j + 1] * inv);
        __builtin_amdgcn_raw_buffer_store_b128(
            of, rs, WS_OF + ((2 * tg + i) * 32 + 4 * hs + hd) * 1024 + lane * 16, 0, 16);
        publish(ws, WS_OFL + ((tg * 8 + hs) * 8 + w) * FL, tag, lane);
        return;
    }

    const int wb4 = w - 8;
    if (kvp) {
        drain();
        lds_arrive(cb + C_BIN);
        if (wb4 < 2) {
            const int tt = 2 * tg + xt, kind = hs >> 1;
            const int c = lane & 15, g = lane >> 4;
            const char* xb = smem + L_X + (32 * xt + 16 * wb4 + c) * 1024;
            const char* wp = smem + L_WKV + (8 * g + tq) * 32 + 8 * tp;
            const int pos = ((g & 1) ? (kind ? 256 : 512) : 0) + (wb4 ? (kind ? 512 : 256) : 0) + c * 16;
            lds_wait<0>(cb + C_BIN, 4);
            const f32x4 a = kind ? kv_proj<false>(xb, wp, g, c) : kv_proj<true>(xb, wp, g, c);
            const unsigned d0 = pack2(a[0], a[1]), d1 = pack2(a[2], a[3]);
            __builtin_amdgcn_raw_buffer_store_b128((u32x4){d0, (unsigned)tag, d1, (unsigned)tag}, rs,
                                                   WS_KVG + tt * 4096 + kind * 2048 + pos + (g >> 1) * 1024, 0, 16);
            __builtin_amdgcn_raw_buffer_store_b64((u32x2){d0, d1}, rs,
                                                  WS_KV + tt * 2048 + kind * 1024 + pos + (g >> 1) * 8, 0, 16);
            lds_arrive(cb + C_KVRD);
            drain();
            if (lds_arrive_count(cb + C_KVST) == 1 && lane < 8)
                __hip_atomic_store((GLBP u64*)(ws + WS_KVF + lane * KVF_REP + (tt * 2 + kind) * 8), tag,
                                   __ATOMIC_RELAXED, __HIP_MEMORY_SCOPE_AGENT);
        }
    }
    lds_wait<1>(cb + C_AIN, UWAIT);
    {
        const __amdgpu_buffer_rsrc_t kr_ = __builtin_amdgcn_make_buffer_rsrc((void*)bk, 0, SEQ * DM * 2, 0x00020000);
        const __amdgpu_buffer_rsrc_t vr_ = __builtin_amdgcn_make_buffer_rsrc((void*)bv, 0, SEQ * DM * 2, 0x00020000);
        const int o0 = (us0 * DM + ud0) * 2;
        int om = o0 - 3072;
        asm volatile("" : "+v"(om));
#pragma unroll
        for (int j = 0; j < 3; ++j) kr[j] = __builtin_amdgcn_raw_buffer_load_b32(kr_, om + 1024 * j, 0, 0);
#pragma unroll
        for (int j = 0; j < 8; ++j) {
            kr[3 + j] = __builtin_amdgcn_raw_buffer_load_b32(kr_, o0 + 1024 * (j & 3), 4096 * (j >> 2), 0);
            vr[j] = __builtin_amdgcn_raw_buffer_load_b32(vr_, o0 + 1024 * (j & 3), 4096 * (j >> 2), 0);
        }
        sk = ld16(shk + ud0 * 4);
    }
    {
        asm volatile("" : "+v"(kr[0]), "+v"(kr[1]), "+v"(kr[2]), "+v"(kr[3]), "+v"(kr[4]), "+v"(kr[5]), "+v"(kr[6]),
                          "+v"(kr[7]), "+v"(kr[8]), "+v"(kr[9]), "+v"(kr[10]), "+v"(sk));
        asm volatile("" : "+v"(vr[0]), "+v"(vr[1]), "+v"(vr[2]), "+v"(vr[3]), "+v"(vr[4]), "+v"(vr[5]), "+v"(vr[6]),
                          "+v"(vr[7]));
        u32x4 ua, ub = {0, 0, 0, 0};
#pragma unroll 1
        for (int e = 0; e < 2; ++e) {
            const unsigned sel = e ? 0x07060c0cu : 0x05040c0cu;
            const unsigned w01 = e ? sk[2] : sk[0], w23 = e ? sk[3] : sk[1];
            const float w0 = bflo(w01), w1 = bfhi(w01), w2 = bflo(w23), w3 = bfhi(w23);
            float kk[11], u[8];
#pragma unroll
            for (int j = 0; j < 11; ++j) kk[j] = __uint_as_float(__builtin_amdgcn_perm(kr[j], 0u, sel));
#pragma unroll
            for (int j = 0; j < 8; j += 2) {
                const unsigned p3 = pack2(kk[j] * w3, kk[j + 1] * w3), p2 = pack2(kk[j + 1] * w2, kk[j + 2] * w2);
                const unsigned p1 = pack2(kk[j + 2] * w1, kk[j + 3] * w1), p0 = pack2(kk[j + 3] * w0, kk[j + 4] * w0);
                unsigned a = pack2(bflo(p3) + bflo(p2), bfhi(p3) + bfhi(p2));
                a = pack2(bflo(a) + bflo(p1), bfhi(a) + bfhi(p1));
                a = pack2(bflo(a) + bflo(p0), bfhi(a) + bfhi(p0));
                u[j] = bflo(a) * __uint_as_float(__builtin_amdgcn_perm(vr[j], 0u, sel));
                u[j + 1] = bfhi(a) * __uint_as_float(__builtin_amdgcn_perm(vr[j + 1], 0u, sel));
            }
            ua = ub;
            ub = (u32x4){pack2(u[7], u[6]), pack2(u[5], u[4]), pack2(u[3], u[2]), pack2(u[1], u[0])};
        }
        lds_wait<1>(cb + C_SEEN, 1);
        lds_wait<1>(cb + C_GATE, 8);
        const int s0 = wb4 < 2 ? 8 : 40, ns = wb4 < 2 ? 32 : 24;
        const char* src = ws + WS_KV + (wb4 & 1) * 1024;
        const unsigned dst = base + ((wb4 & 1) ? L_V : L_K);
#pragma unroll 1
        for (int t = s0; t < s0 + ns; ++t) dma16_dev(src + ((t + rot) & 63) * 2048, lane * 16, dst + t * 1024);
        if (wb4 < 2) {
            asm volatile("s_waitcnt vmcnt(24)" ::: "memory");
            lds_arrive(cb + C_NEXT);
        }
        __builtin_amdgcn_raw_buffer_store_b128(ua, rs, WS_UR + (ud0 * SEQ + (SEQ - 8) - us0) * 2, 0, 16);
        __builtin_amdgcn_raw_buffer_store_b128(ub, rs, WS_UR + (ud0 * SEQ + (SEQ - 8) - us0) * 2, SEQ * 2, 16);
        drain();
        lds_arrive(cb + (wb4 < 2 ? C_MID : C_LAST));
        if (lds_arrive_count(cb + C_UR) == 3) {
            lds_wait<1>(cb + C_TAG, 8);
            if (lane == 0)
                __hip_atomic_store((GLBP u64*)(ws + WS_URF + (hs * 32 + tg) * 8), tag, __ATOMIC_RELAXED, __HIP_MEMORY_SCOPE_AGENT);
        }
        lds_wait<1>(cb + C_FIRST, 8);
    }

    {
        const int ch = wb4 >> 1, sh = wb4 & 1;
        const int d = 2 * blk + ch;
        const float dd = bf2f(ssmd[d]);
        if (wb4 == 0) {
            wait_flags<16>(ws, WS_URF + ((d >> 6) * 32 + lane) * 8, lane < 32, tag);
            lds_arrive(cb + C_URS);
        }
        lds_wait<4>(cb + C_URS, 1);
        char* ul = smem + L_U + ch * 2 * U_RS;
        u32x4 uv[4];
#pragma unroll
        for (int j = 0; j < 4; ++j)
            uv[j] = __builtin_amdgcn_raw_buffer_load_b128(rs, WS_UR + d * 4096 + j * 1024 + lane * 16 + 2 * sh, 0, 16);
        if (sh && lane == 63) uv[3][3] &= 0xffffu;
#pragma unroll
        for (int j = 0; j < 4; ++j)
            *reinterpret_cast<u32x4*>(ul + sh * U_RS + j * 1024 + lane * 16) = uv[j];
        if (lane < 2) *reinterpret_cast<u32x4*>(ul + sh * U_RS + 4096 + lane * 16) = (u32x4){0, 0, 0, 0};
        lds_arrive(cb + C_U + 4 * ch);
        lds_wait<1>(cb + C_U + 4 * ch, 2);
        const __amdgpu_buffer_rsrc_t hk = __builtin_amdgcn_make_buffer_rsrc((void*)(ssmk + (size_t)d * SEQ), 0, SEQ * 2, 0x00020000);
        if (sh) conv_wave<1, 2, 4, 7>(hk, ul, rs, WS_ZT + d * 4096, dd, lane);
        else conv_wave<0, 3, 5, 6>(hk, ul, rs, WS_ZT + d * 4096, dd, lane);
        publish(ws, WS_ZF + (((d >> 6) * 2 + sh) * 64 + (d & 63)) * 8, tag, lane);
    }

    if (blk == 0 && wb4 == 2) {
#pragma unroll 1
        for (int j = 0; j < 4; ++j) wait_flags<24>(ws, WS_URF + (j * 64 + lane) * 8, true, tag);
        if (lane == 0) __hip_atomic_store((GLBP u64*)(ws + WS_EPOCH), tag, __ATOMIC_RELAXED, __HIP_MEMORY_SCOPE_AGENT);
    }

    u32x4 fe[8], fo[8];
    {
        const __amdgpu_buffer_rsrc_t wr = __builtin_amdgcn_make_buffer_rsrc((void*)wo, 0, DM * DM * 2, 0x00020000);
        const int vo = (4 * h * DM + 2 * (r ^ (8 * wb4))) * 2;
        const int so = (128 * wb4 * DM + 64 * hs) * 2;
        unsigned wd[8][8];
#pragma unroll
        for (int s = 0; s < 8; ++s)
#pragma unroll
            for (int j = 0; j < 8; ++j)
                wd[s][j] = __builtin_amdgcn_raw_buffer_load_b32(wr, vo + (j & 3) * 1024, so + (16 * s + 8 * (j >> 2)) * 1024, 0);
        __builtin_amdgcn_sched_barrier(0);
#pragma unroll
        for (int s = 0; s < 8; ++s) {
#pragma unroll
            for (int j = 0; j < 4; ++j) {
                fe[s][j] = __builtin_amdgcn_perm(wd[s][2 * j + 1], wd[s][2 * j], 0x05040100u);
                fo[s][j] = __builtin_amdgcn_perm(wd[s][2 * j + 1], wd[s][2 * j], 0x07060302u);
            }
            asm volatile("" : "+v"(fe[s]), "+v"(fo[s]));
        }
    }
    {
        const int s0 = 64 * tg + 16 * wb4 + 2 * (lane >> 3), d0 = 64 * hs + 8 * (lane & 7);
        const u32x4 q0 = ld16(bq + (size_t)s0 * DM + d0), q1 = ld16(bq + (size_t)(s0 + 1) * DM + d0);
        if (wb4 == 0) {
            wait_flags<16>(ws, WS_ZF + ((hs * 2 + ((0x96 >> (tg >> 2)) & 1)) * 64 + lane) * 8, true, tag);
            lds_arrive(cb + C_Z);
        }
        lds_wait<4>(cb + C_Z, 1);
        unsigned z[8];
#pragma unroll
        for (int e = 0; e < 8; ++e)
            z[e] = __builtin_amdgcn_raw_buffer_load_b32(rs, (d0 * SEQ + s0) * 2, WS_ZT + e * SEQ * 2, 16);
        __builtin_amdgcn_sched_barrier(0);
        u32x4 o0, o1;
#pragma unroll
        for (int j = 0; j < 4; ++j) {
            o0[j] = pack2(bflo(q0[j]) * bflo(z[2 * j]), bfhi(q0[j]) * bflo(z[2 * j + 1]));
            o1[j] = pack2(bflo(q1[j]) * bfhi(z[2 * j]), bfhi(q1[j]) * bfhi(z[2 * j + 1]));
        }
        __builtin_amdgcn_raw_buffer_store_b128(o0, ro, (s0 * 1024 + 512 + d0) * 2, 0, OUT_AUX);
        __builtin_amdgcn_raw_buffer_store_b128(o1, ro, (s0 * 1024 + 512 + d0) * 2 + 2048, 0, OUT_AUX);
    }
    lds_arrive(cb + C_TAIL);
    lds_wait<8>(cb + C_NEAR, 4);
#pragma unroll 1
    for (int i = 0; i < 2; ++i) {
        wait_flags<1>(ws, WS_OFL + ((tg * 8 + 2 * wb4 + (lane >> 2)) * 8 + 4 * i + (lane & 3)) * FL, lane < 8, tag);
        u32x4 of[8];
#pragma unroll
        for (int s = 0; s < 8; ++s)
            of[s] = __builtin_amdgcn_raw_buffer_load_b128(
                rs, lane * 16 + (s & 3) * 1024, WS_OF + ((2 * tg + i) * 32 + 8 * wb4 + (s & 4)) * 1024, 16);
        __builtin_amdgcn_sched_barrier(0);
        f32x16 ae = {0}, ao = {0};
#pragma unroll
        for (int s = 0; s < 8; ++s) {
            ae = mfma32(fe[s], of[s], ae);
            ao = mfma32(fo[s], of[s], ao);
        }
        lds_wait<0>(cb + C_TAIL, 4 + 8 * i);
#pragma unroll
        for (int g = 1; g < 4; ++g) {
            f32x4* p = reinterpret_cast<f32x4*>(smem + L_RED + ((g ^ wb4) * 3 + g - 1) * 2048) + lane;
            p[0] = (f32x4){ae[4 * g], ae[4 * g + 1], ae[4 * g + 2], ae[4 * g + 3]};
            p[64] = (f32x4){ao[4 * g], ao[4 * g + 1], ao[4 * g + 2], ao[4 * g + 3]};
        }
        lds_arrive(cb + C_TAIL);
        lds_wait<0>(cb + C_TAIL, 8 + 8 * i);
        f32x4 e = {ae[0], ae[1], ae[2], ae[3]}, o = {ao[0], ao[1], ao[2], ao[3]};
        const f32x4* p = reinterpret_cast<const f32x4*>(smem + L_RED + wb4 * 3 * 2048) + lane;
#pragma unroll
        for (int g = 0; g < 3; ++g) {
            e += p[g * 128];
            o += p[g * 128 + 64];
        }
        const u32x4 res = {pack2(e[0], o[0]), pack2(e[1], o[1]), pack2(e[2], o[2]), pack2(e[3], o[3])};
        __builtin_amdgcn_raw_buffer_store_b128(res, ro, ((64 * tg + 32 * i + r) * 1024 + 64 * hs + 16 * wb4 + 8 * h) * 2, 0, OUT_AUX);
        lds_arrive(cb + C_TAIL);
    }
}

extern "C" void kernel_launch(void* const* d_in, const int* in_sizes, int n_in, void* d_out,
                              int out_size, void* d_ws, size_t ws_size, hipStream_t stream) {
    (void)hipFuncSetAttribute((const void*)fused_kernel, hipFuncAttributeMaxDynamicSharedMemorySize, LDS_BYTES);
    fused_kernel<<<256, 768, LDS_BYTES, stream>>>(
        (const u16*)d_in[0], (const u16*)d_in[1], (const u16*)d_in[2], (const u16*)d_in[3],
        (const u16*)d_in[4], (const u16*)d_in[5], (const u16*)d_in[6], (const u16*)d_in[7],
        (const u16*)d_in[8], (const u16*)d_in[9], (const u16*)d_in[10], (u16*)d_out, (char*)d_ws);
}
```

```cpp
#include <hip/hip_runtime.h>
#include <hip/hip_bf16.h>
#define OUT_AUX 0
#define UWAIT 8
#define APRIO 2
#define NW1 7
#define EV2 12
#define EV3 6
#define USLEEP 28
#define CSP 3

typedef __attribute__((ext_vector_type(8))) __bf16 bf16x8;
typedef __attribute__((ext_vector_type(2))) __bf16 bf16x2;
typedef __attribute__((ext_vector_type(16))) float f32x16;
typedef __attribute__((ext_vector_type(4))) float f32x4;
typedef __attribute__((ext_vector_type(2))) float f32x2;
typedef __attribute__((ext_vector_type(4))) unsigned u32x4;
typedef __attribute__((ext_vector_type(2))) unsigned u32x2;
typedef __attribute__((ext_vector_type(4))) short s16x4;
typedef unsigned long long u64;
typedef unsigned short u16;
#define LDSP __attribute__((address_space(3)))
#define GLBP __attribute__((address_space(1)))

constexpr int SEQ = 2048;
constexpr int DM = 512;

constexpr int FL = 128;
constexpr int WS_EPOCH = 0;
constexpr int KVF_REP = 1024 + 4096;
constexpr int WS_KVF = 4096;
constexpr int WS_URF = WS_KVF + 8 * KVF_REP;
constexpr int WS_ZF = WS_URF + 4096;
constexpr int WS_OFL = WS_ZF + 8192;
constexpr int WS_KV = WS_OFL + 2048 * FL;
constexpr int WS_KVG = WS_KV + 131072;
constexpr int WS_UR = WS_KVG + 262144;
constexpr int WS_ZT = WS_UR + 2097152 + 4096;
constexpr int WS_OF = WS_ZT + 2097152;
constexpr int WS_END = WS_OF + 2097152;

constexpr int L_X = 0;
constexpr int L_W = 65536;
constexpr int L_WKV = 147456;
constexpr int L_K = 0;
constexpr int L_V = 65536;
constexpr int L_CONST = 131072;
constexpr int L_CNT = L_CONST + 9 * 512;
constexpr int L_U = L_CNT + 128;
constexpr int U_RS = 4160;
constexpr int L_RED = L_U;
constexpr int L_QX = 147456;
constexpr int LDS_BYTES = 163840;
constexpr int C_FIRST = 0;
constexpr int C_MID = 4;
constexpr int C_NEAR = 8;
constexpr int C_BIN = 12;
constexpr int C_U = 16;
constexpr int C_KVRD = 24;
constexpr int C_Z = 64;
constexpr int C_TAG = 60;
constexpr int C_KVST = 56;
constexpr int C_AIN = 28;
constexpr int C_TAIL = 32;
constexpr int C_GATE = 36;
constexpr int C_LAST = 40;
constexpr int C_NEXT = 44;
constexpr int C_SEEN = 48;
constexpr int C_URS = 68;
constexpr int C_UR = 52;

constexpr float QSCALE = 0.25f * 1.4426950408889634f;
constexpr int CODE_BYTES = 12288;
constexpr unsigned SPIN_CAP = 1u << 18;

__device__ __forceinline__ float bf2f(u16 v) { return __uint_as_float(((unsigned)v) << 16); }
__device__ __forceinline__ float bflo(unsigned v) { return __uint_as_float(v << 16); }
__device__ __forceinline__ float bfhi(unsigned v) { return __uint_as_float(v & 0xffff0000u); }
__device__ __forceinline__ unsigned pack2(float lo, float hi) {
    f32x2 f = {lo, hi};
    bf16x2 b = __builtin_convertvector(f, bf16x2);
    return __builtin_bit_cast(unsigned, b);
}
__device__ __forceinline__ float rbf(float x) { return (float)(__bf16)x; }
__device__ __forceinline__ bf16x8 as_bf(u32x4 v) { return __builtin_bit_cast(bf16x8, v); }
__device__ __forceinline__ f32x16 mfma32(u32x4 a, u32x4 b, f32x16 c) {
    return __builtin_amdgcn_mfma_f32_32x32x16_bf16(as_bf(a), as_bf(b), c, 0, 0, 0);
}
__device__ __forceinline__ f32x4 mfma16(u32x4 a, u32x4 b, f32x4 c) {
    return __builtin_amdgcn_mfma_f32_16x16x32_bf16(as_bf(a), as_bf(b), c, 0, 0, 0);
}
__device__ __forceinline__ u32x4 ld16(const void* p) { return *reinterpret_cast<const u32x4*>(p); }
__device__ __forceinline__ u32x2 ld8(const void* p) { return *reinterpret_cast<const u32x2*>(p); }
__device__ __forceinline__ u32x4 tr_frag(const char* p, int second) {
    const s16x4 a = __builtin_amdgcn_ds_read_tr16_b64_v4i16((LDSP s16x4*)p);
    const s16x4 b = __builtin_amdgcn_ds_read_tr16_b64_v4i16((LDSP s16x4*)(p + second));
    const u32x2 a2 = __builtin_bit_cast(u32x2, a), b2 = __builtin_bit_cast(u32x2, b);
    return (u32x4){a2[0], a2[1], b2[0], b2[1]};
}
__device__ __forceinline__ void dma16(const void* src, unsigned voff, unsigned dst) {
    asm volatile("s_mov_b32 m0, %2\n\ts_nop 0\n\tglobal_load_lds_dwordx4 %0, %1" ::"v"(voff), "s"(src), "s"(dst) : "memory");
}
__device__ __forceinline__ void dma16_dev(const void* src, unsigned voff, unsigned dst) {
    asm volatile("s_mov_b32 m0, %2\n\ts_nop 0\n\tglobal_load_lds_dwordx4 %0, %1 sc1" ::"v"(voff), "s"(src), "s"(dst) : "memory");
}
__device__ __forceinline__ void barrier() {
    asm volatile("s_waitcnt lgkmcnt(0)\n\ts_barrier" ::: "memory");
}
__device__ __forceinline__ void drain() { asm volatile("s_waitcnt vmcnt(0)" ::: "memory"); }

__device__ __forceinline__ void lds_arrive(unsigned addr) {
    u64 sx;
    asm volatile("s_waitcnt lgkmcnt(0)\n\ts_mov_b64 %0, exec\n\ts_mov_b64 exec, 1\n\t"
                 "ds_add_u32 %1, %2\n\ts_mov_b64 exec, %0"
                 : "=&s"(sx) : "v"(addr), "v"(1u) : "memory");
}
__device__ __forceinline__ unsigned lds_arrive_count(unsigned addr) {
    u64 sx;
    unsigned v, st;
    asm volatile("s_waitcnt lgkmcnt(0)\n\ts_mov_b64 %0, exec\n\ts_mov_b64 exec, 1\n\t"
                 "ds_add_rtn_u32 %1, %3, %4\n\ts_waitcnt lgkmcnt(0)\n\tv_readfirstlane_b32 %2, %1\n\ts_mov_b64 exec, %0"
                 : "=&s"(sx), "=&v"(v), "=&s"(st) : "v"(addr), "v"(1u) : "memory");
    return st;
}
template <int SLEEP>
__device__ __forceinline__ void lds_wait(unsigned addr, unsigned n) {
    unsigned v, st;
    asm volatile("s_branch 2f\n1:\n\ts_sleep %4\n2:\n\tds_read_b32 %0, %2\n\ts_waitcnt lgkmcnt(0)\n\t"
                 "v_readfirstlane_b32 %1, %0\n\ts_cmp_lt_u32 %1, %3\n\ts_cbranch_scc1 1b"
                 : "=&v"(v), "=&s"(st) : "v"(addr), "s"(n), "i"(SLEEP) : "scc", "memory");
}

__device__ __forceinline__ void publish(char* ws, int off, u64 tag, int lane) {
    drain();
    if (lane == 0) __hip_atomic_store((GLBP u64*)(ws + off), tag, __ATOMIC_RELAXED, __HIP_MEMORY_SCOPE_AGENT);
}
template <int SLEEP>
__device__ __forceinline__ void wait_flags(char* ws, int off, bool active, u64 tag) {
#pragma unroll 1
    for (unsigned spin = 0; spin < SPIN_CAP; ++spin) {
        u64 v = tag;
        if (active) v = __hip_atomic_load((GLBP u64*)(ws + off), __ATOMIC_RELAXED, __HIP_MEMORY_SCOPE_AGENT);
        if (__all(v == tag)) break;
        __builtin_amdgcn_s_sleep(SLEEP);
    }
    asm volatile("" ::: "memory");
}

#define STR2(x) #x
#define STR(x) STR2(x)
#define EX(r) "v_exp_f32 v" STR(r) ", v" STR(r) "\n"
#define CV(d, a, b) "v_cvt_pk_bf16_f32 v" STR(d) ", v" STR(a) ", v" STR(b) "\n"
#define TILE(x0, x1, x2, x3, x4, x5, x6, x7, x8, x9, x10, x11, x12, x13, x14, x15, NEXT, u, VA, uv) \
    "s_waitcnt lgkmcnt(2)\n"                                                                    \
    "v_mfma_f32_32x32x16_bf16 " NEXT ", v[104:107], %[q], v[116:131]\n"                         \
    "ds_read_b128 v[104:107], %[ka] offset:" STR((u + 2) * 1024) "\n"                           \
    EX(x0) EX(x1) EX(x2) EX(x3) CV(96, x0, x1) EX(x4) EX(x5) CV(97, x2, x3)                     \
    "s_waitcnt lgkmcnt(2)\n"                                                                    \
    "v_mfma_f32_32x32x16_bf16 %[o], v[112:115], v[100:103], %[o]\n"                             \
    "ds_read_b128 v[112:115], " VA " offset:" STR((2 * uv + 1) * 512) "\n"                      \
    EX(x6) EX(x7) CV(98, x4, x5) EX(x8) CV(99, x6, x7) EX(x9) EX(x10) EX(x11)                   \
    "s_waitcnt lgkmcnt(2)\n"                                                                    \
    "v_mfma_f32_32x32x16_bf16 %[o], v[108:111], v[96:99], %[o]\n"                               \
    "ds_read_b128 v[108:111], " VA " offset:" STR((2 * uv + 2) * 512) "\n"                      \
    EX(x12) EX(x13) CV(100, x8, x9) EX(x14) EX(x15) CV(101, x10, x11) CV(102, x12, x13)         \
    CV(103, x14, x15)
#define TILE_X(u, VA, uv) TILE(64, 65, 66, 67, 68, 69, 70, 71, 72, 73, 74, 75, 76, 77, 78, 79, "v[80:95]", u, VA, uv)
#define TILE_Y(u, VA, uv) TILE(80, 81, 82, 83, 84, 85, 86, 87, 88, 89, 90, 91, 92, 93, 94, 95, "v[64:79]", u, VA, uv)
#define MOVC(r) "v_mov_b32 v" STR(r) ", v116\n"
#define LDS_WAIT(off, n, lbl)                                                                   \
    lbl ":\n"                                                                                   \
    "ds_read_b32 v96, %[ca] offset:" STR(off) "\n"                                              \
    "s_waitcnt lgkmcnt(0)\n"                                                                    \
    "v_readfirstlane_b32 %[st], v96\n"                                                          \
    "s_cmp_lt_u32 %[st], " STR(n) "\n"                                                          \
    "s_cbranch_scc1 " lbl "b\n"
#define LDS_ARRIVE(off)                                                                         \
    "s_mov_b64 %[sx], exec\n"                                                                   \
    "s_mov_b64 exec, 1\n"                                                                       \
    "v_mov_b32 v96, 1\n"                                                                        \
    "ds_add_u32 %[ca], v96 offset:" STR(off) "\n"                                               \
    "s_mov_b64 exec, %[sx]\n"
#define ATT_CLOB                                                                                \
    "v64", "v65", "v66", "v67", "v68", "v69", "v70", "v71", "v72", "v73", "v74", "v75", "v76",  \
    "v77", "v78", "v79", "v80", "v81", "v82", "v83", "v84", "v85", "v86", "v87", "v88", "v89",  \
    "v90", "v91", "v92", "v93", "v94", "v95", "v96", "v97", "v98", "v99", "v100", "v101",       \
    "v102", "v103", "v104", "v105", "v106", "v107", "v108", "v109", "v110", "v111", "v112",     \
    "v113", "v114", "v115", "v116", "v117", "v118", "v119", "v120", "v121", "v122", "v123",     \
    "v124", "v125", "v126", "v127", "v128", "v129", "v130", "v131", "v132", "v133"

#define ITER_STAMP
#define ITER_OUT
#define ITER_CLOB
__device__ __forceinline__ f32x16 attn_pass(unsigned ka, unsigned va, unsigned vstep, unsigned ca,
                                            u32x4 q, float mgiven, int given) {
    f32x16 o = {0};
    int cnt, ev, st;
    u64 sx;
    asm volatile(
        "ds_read_b128 v[104:107], %[ka]\n"
        "v_mov_b32 v116, %[mg]\n"
        "s_cmp_lg_u32 %[gv], 0\n"
        "s_waitcnt lgkmcnt(0)\n"
        "s_cbranch_scc1 11f\n"
        "v_mfma_f32_32x32x16_bf16 v[64:79], v[104:107], %[q], 0\n"
        "s_nop 11\n"
        "v_max3_f32 v96, v64, v65, v66\n"
        "v_max3_f32 v97, v67, v68, v69\n"
        "v_max3_f32 v98, v70, v71, v72\n"
        "v_max3_f32 v99, v73, v74, v75\n"
        "v_max3_f32 v96, v96, v76, v77\n"
        "v_max3_f32 v97, v97, v78, v79\n"
        "v_max3_f32 v96, v96, v98, v99\n"
        "v_max_f32 v96, v96, v97\n"
        "v_mov_b32 v97, v96\n"
        "s_nop 1\n"
        "v_permlane32_swap_b32 v96, v97\n"
        "s_nop 1\n"
        "v_max_f32 v116, v96, v97\n"
        "11:\n"
        "v_xor_b32 v116, 0x80000000, v116\n"
        MOVC(117) MOVC(118) MOVC(119) MOVC(120) MOVC(121) MOVC(122) MOVC(123) MOVC(124)
        MOVC(125) MOVC(126) MOVC(127) MOVC(128) MOVC(129) MOVC(130) MOVC(131)
        "v_mov_b32 v100, 0\n v_mov_b32 v101, 0\n v_mov_b32 v102, 0\n v_mov_b32 v103, 0\n"
        "s_movk_i32 %[cnt], 16\n"
        "s_setprio 2\n"
        "v_mfma_f32_32x32x16_bf16 v[64:79], v[104:107], %[q], v[116:131]\n"
        "ds_read_b128 v[104:107], %[ka] offset:1024\n"
        "ds_read_b128 v[112:115], %[va]\n"
        "ds_read_b128 v[108:111], %[va]\n"
        "s_nop 10\n"
        "s_movk_i32 %[ev], 16\n"
        "v_add_u32 v132, %[vs], %[va]\n"
        "v_add_u32 v133, %[vs], %[vs]\n"
        "12:\n"
        TILE_X(0, "%[va]", 0) TILE_Y(1, "%[va]", 1) TILE_X(2, "%[va]", 2) TILE_Y(3, "%[va]", 3)
        "s_cmp_eq_u32 %[cnt], %[ev]\n"
        "s_cbranch_scc1 14f\n"
        "13:\n"
        TILE_X(4, "v132", 0)
        "v_add_u32 %[va], v133, %[va]\n"
        TILE_Y(5, "v132", 1) TILE_X(6, "v132", 2) TILE_Y(7, "v132", 3)
        "v_add_u32 %[ka], 8192, %[ka]\n"
        "v_add_u32 v132, v133, v132\n"
        ITER_STAMP
        "s_sub_u32 %[cnt], %[cnt], 2\n"
        "s_cmp_lg_u32 %[cnt], 0\n"
        "s_cbranch_scc1 12b\n"
        "s_branch 18f\n"
        "14:\n"
        "s_cmp_eq_u32 %[ev], 16\n"
        "s_cbranch_scc0 21f\n"
        LDS_WAIT(44, 2, "22")
        "s_movk_i32 %[ev], 14\n"
        "s_branch 13b\n"
        "21:\n"
        "s_cmp_eq_u32 %[ev], 14\n"
        "s_cbranch_scc0 15f\n"
        LDS_WAIT(4, 2, "17")
        "s_movk_i32 %[ev], " STR(EV2) "\n"
        "s_branch 13b\n"
        "15:\n"
        "s_cmp_eq_u32 %[ev], " STR(EV2) "\n"
        "s_cbranch_scc0 16f\n"
        LDS_WAIT(40, 2, "19")
        "s_setprio 1\n"
        "s_movk_i32 %[ev], " STR(EV3) "\n"
        "s_branch 13b\n"
        "16:\n"
        "s_cmp_eq_u32 %[ev], 2\n"
        "s_movk_i32 %[ev], 2\n"
        "s_setprio 0\n"
        "s_cbranch_scc0 13b\n"
        LDS_ARRIVE(8)
        "s_waitcnt lgkmcnt(0)\n"
        "s_branch 13b\n"
        "18:\n"
        "s_waitcnt lgkmcnt(0)\n"
        "v_mfma_f32_32x32x16_bf16 %[o], v[112:115], v[100:103], %[o]\n"
        "s_nop 15\n"
        : [o] "+v"(o), [ka] "+v"(ka), [va] "+v"(va), [cnt] "=&s"(cnt), [ev] "=&s"(ev), [st] "=&s"(st), [sx] "=&s"(sx) ITER_OUT
        : [q] "v"(q), [vs] "v"(vstep), [ca] "v"(ca), [mg] "v"(mgiven), [gv] "s"(given)
        : ATT_CLOB, "scc", "memory" ITER_CLOB);
    return o;
}

__device__ __forceinline__ float row_max(const char* kb, u32x4 q) {
    float m = -__builtin_inff();
    const f32x16 z = {0};
    for (int t = 0; t < 64; ++t) {
        const f32x16 s = mfma32(ld16(kb + t * 1024), q, z);
#pragma unroll
        for (int j = 0; j < 16; ++j) m = fmaxf(m, s[j]);
    }
    return fmaxf(m, __shfl_xor(m, 32));
}

#define GAP __builtin_amdgcn_sched_barrier(0); asm volatile("s_sleep 1"); __builtin_amdgcn_sched_barrier(0);
template <int T0, int T1, int T2, int T3>
__device__ __forceinline__ void conv_wave(__amdgpu_buffer_rsrc_t hr, const char* ul,
                                          __amdgpu_buffer_rsrc_t rs, int zoff, float dd, int lane) {
    constexpr int NF = T3 + 1;
    const int r = lane & 15, g = lane >> 4;
    const int ns = (SEQ - 1) - 16 * (g >> 1) - r + 8 * (g & 1);
    unsigned ap = (unsigned)(size_t)(LDSP const char*)ul + ((ns & 1) ? (U_RS + 2 * (ns - 1)) : 2 * ns) - 512 * (NF - 1);
    int b0 = 2 * (16 * r - 16 * (g >> 1) + 8 * (g & 1));
    f32x4 c0 = {0}, c1 = {0}, c2 = {0}, c3 = {0};
#pragma unroll 1
    for (int rho = 0; rho < 8; ++rho) {
        u32x4 A[NF], B[NF];
        int b1 = b0 + 512;
        asm volatile("" : "+v"(b1), "+v"(ap));
        B[0] = __builtin_amdgcn_raw_buffer_load_b128(hr, b0, 0, 0);
#pragma unroll
        for (int b = 1; b < NF; ++b) B[b] = __builtin_amdgcn_raw_buffer_load_b128(hr, b1 + 512 * (b - 1), 0, 0);
#pragma unroll
        for (int a = 0; a < NF; ++a) {
            volatile LDSP const unsigned* p = (volatile LDSP const unsigned*)(size_t)(ap + 512 * (NF - 1 - a));
            A[a] = (u32x4){p[0], p[1], p[2], p[3]};
        }
        __builtin_amdgcn_sched_barrier(0);
#pragma unroll
        for (int a = 0; a < NF; ++a) {
            c3 = mfma16(A[a], B[T3 - a], c3);
            GAP
            if (a <= T2) { c2 = mfma16(A[a], B[a <= T2 ? T2 - a : 0], c2); GAP }
            if (a <= T1) { c1 = mfma16(A[a], B[a <= T1 ? T1 - a : 0], c1); GAP }
            if (a <= T0) { c0 = mfma16(A[a], B[a <= T0 ? T0 - a : 0], c0); GAP }
        }
        b0 -= 64;
        ap -= 64;
    }
    const int ts[4] = {T0, T1, T2, T3};
    const f32x4 cs[4] = {c0, c1, c2, c3};
#pragma unroll
    for (int i = 0; i < 4; ++i) {
        const int s = 256 * ts[i] + 16 * r + 4 * g;
        const u32x2 uu = ld8(ul + 2 * ((SEQ - 4) - s));
        const u32x2 o = {pack2(rbf(cs[i][0]) + rbf(bfhi(uu[1]) * dd), rbf(cs[i][1]) + rbf(bflo(uu[1]) * dd)),
                         pack2(rbf(cs[i][2]) + rbf(bfhi(uu[0]) * dd), rbf(cs[i][3]) + rbf(bflo(uu[0]) * dd))};
        __builtin_amdgcn_raw_buffer_store_b64(o, rs, zoff + 2 * s, 0, 16);
    }
}

template <bool WFIRST>
__device__ __forceinline__ f32x4 kv_proj(const char* xb, const char* wp, int g, int c) {
    u32x4 fw[16], fx[16];
    f32x4 a = {0};
    asm volatile("" ::: "memory");
#define KV_LOAD(G)                                                                   \
    _Pragma("unroll") for (int s = 4 * G; s < 4 * G + 4; ++s) {                      \
        fw[s] = tr_frag(wp + s * 1024, 128);                                         \
        fx[s] = ld16(xb + G * 256 + (((4 * (s & 3) + g) ^ c) << 4));                 \
    }                                                                                \
    __builtin_amdgcn_sched_barrier(0);
#define KV_MMA(G)                                                                    \
    _Pragma("unroll") for (int s = 4 * G; s < 4 * G + 4; ++s)                        \
        a = WFIRST ? mfma16(fw[s], fx[s], a) : mfma16(fx[s], fw[s], a);              \
    __builtin_amdgcn_sched_barrier(0);
    KV_LOAD(0) KV_LOAD(1) KV_LOAD(2) KV_MMA(0) KV_LOAD(3) KV_MMA(1) KV_MMA(2) KV_MMA(3)
    return a;
}

__global__ void __launch_bounds__(768)
fused_kernel(const u16* __restrict__ x, const u16* __restrict__ wq, const u16* __restrict__ wk,
             const u16* __restrict__ wv, const u16* __restrict__ wo, const u16* __restrict__ bq,
             const u16* __restrict__ bk, const u16* __restrict__ bv, const u16* __restrict__ shk,
             const u16* __restrict__ ssmk, const u16* __restrict__ ssmd, u16* __restrict__ out,
             char* ws) {
    extern __shared__ __attribute__((aligned(16))) char smem[];
    const int lane = threadIdx.x & 63;
    const int w = __builtin_amdgcn_readfirstlane(threadIdx.x >> 6);
    const int blk = blockIdx.x;
    const int tg = (blk & 7) * 4 + (blk >> 6);
    const int hs = (blk >> 3) & 7;
    const int r = lane & 31, h = lane >> 5;
    const __amdgpu_buffer_rsrc_t rs = __builtin_amdgcn_make_buffer_rsrc(ws, 0, WS_END, 0x00020000);
    asm volatile("" ::"s"(x), "s"(wq), "s"(wk), "s"(wv), "s"(wo), "s"(bq), "s"(bk), "s"(bv), "s"(shk),
                 "s"(ssmk), "s"(ssmd), "s"(out), "s"(ws));
    const __amdgpu_buffer_rsrc_t ro = __builtin_amdgcn_make_buffer_rsrc(out, 0, SEQ * 1024 * 2, 0x00020000);

    const bool kvp = !(hs & 2);
    const int xt = hs & 1;
    const unsigned base = (unsigned)(size_t)(LDSP char*)smem;
    const unsigned cb = base + L_CNT;
    const int rot = 8 * (blk & 7);
    const u16* xsrc = x + (size_t)(64 * tg) * DM;
    const int us0 = 64 * tg + 8 * (lane >> 3), ud0 = 64 * hs + 16 * (w & 3) + 2 * (lane & 7);
    unsigned kr[11], vr[8];
    u32x4 sk;
    if (w >= 8) {
        __builtin_amdgcn_s_setprio(3);
        const int wb4 = w - 8;
        if (kvp) {
#pragma unroll 1
            for (int row = 32 * xt + wb4; row < 32 * xt + 32; row += 4)
                dma16(xsrc + row * DM, (lane ^ (row & 15)) << 4, base + L_X + row * 1024);
            const u16* src = hs < 4 ? wk : wv;
#pragma unroll 1
            for (int j = wb4; j < 16; j += 4) dma16(src + j * 512, lane * 16, base + L_WKV + j * 1024);
        }
        if (wb4 == 0) {
            const int b3 = lane / 3, p = lane - 3 * b3;
            const unsigned v = (lane < 27 && p == 0) ? 0x3f803f80u : 0u;
            const unsigned dst = lane < 27 ? base + L_CONST + b3 * 512 + (p == 2 ? 64 : 16 * p) : cb + ((lane - 27) & 7) * 16;
            asm volatile("ds_write_b128 %0, %1" ::"v"(dst), "v"((u32x4){v, v, v, v}) : "memory");
        }
        barrier();
        if (hs == 6 && wb4 == 3) {
            const char* pc;
            asm volatile("s_getpc_b64 %0" : "=s"(pc));
            const GLBP unsigned* cp = (const GLBP unsigned*)(pc + lane * 64);
            unsigned sink = 0;
#pragma unroll
            for (int j = 0; j < CODE_BYTES / 4096; ++j) sink += cp[j * 1024];
            asm volatile("" ::"v"(sink));
        }
    } else {
        barrier();
        const u16* wsrc = wq + (size_t)(64 * w) * DM + 64 * hs;
        const unsigned wvo = (lane >> 3) * 1024 + (((lane & 7) ^ (((lane >> 4) & 1) << 2)) << 4);
        const int r0 = kvp ? 32 * (xt ^ 1) : 0, r1 = kvp ? r0 + 32 : 64;
#pragma unroll 1
        for (int row = r0 + w; row < r1; row += 8)
            dma16(xsrc + row * DM, (lane ^ (row & 15)) << 4, base + L_X + row * 1024);
#pragma unroll 1
        for (int j = 0; j < 8; ++j) {
            if (kvp && j == NW1) lds_wait<1>(cb + C_KVRD, 2);
            dma16(wsrc + j * 8 * DM, wvo, base + L_W + (8 * w + j) * 1024);
        }
    }
    u64 tag;
    asm volatile("s_load_dwordx2 %0, %1, 0x0\n\ts_waitcnt lgkmcnt(0)" : "=s"(tag) : "s"(ws) : "memory");
    tag += 1;

    const int tq = (lane & 15) >> 2, tp = lane & 3;
    if (w < 8) {
        lds_arrive(cb + C_TAG);
        __builtin_amdgcn_s_setprio(APRIO);
        const int i = w >> 2, hd = w & 3, kh = hd & 1;
        const int chunk = 4 * (hd >> 1) + 2 * ((lane >> 4) & 1) + (tp >> 1);
        const char* wb = smem + L_W + kh * 32768 + (8 * h + tq) * 128 + ((chunk ^ ((tq >> 1) << 2)) << 4) + 8 * (tp & 1);
        const char* xa[8];
#pragma unroll
        for (int s = 0; s < 8; ++s) {
            unsigned o = L_X + (32 * i + r) * 1024 + kh * 512 + (((2 * s + h) ^ (r & 15)) << 4);
            asm volatile("" : "+v"(o));
            xa[s] = smem + o;
        }
        drain();
        lds_arrive(cb + C_AIN);
        lds_wait<0>(cb + C_AIN, 8);
        f32x16 acc = {0};
        u32x4 fa[16], fb[16];
#define Q_LOAD(G)                                                                    \
    _Pragma("unroll") for (int s = 4 * G; s < 4 * G + 4; ++s) {                      \
        fa[s] = tr_frag(wb + s * 2048, 512);                                         \
        fb[s] = ld16(xa[s & 7] + (s >> 3) * 256);                                    \
    }                                                                                \
    __builtin_amdgcn_sched_barrier(0);
#define Q_MMA(G)                                                                     \
    _Pragma("unroll") for (int s = 4 * G; s < 4 * G + 4; ++s) acc = mfma32(fa[s], fb[s], acc); \
    __builtin_amdgcn_sched_barrier(0);
        const int goff = WS_KVG + ((w + rot) & 63) * 4096 + lane * 16;
        u32x4 gr[4];
        Q_LOAD(0) Q_LOAD(1) Q_MMA(0) Q_LOAD(2) Q_MMA(1) Q_LOAD(3)
#pragma unroll
        for (int j = 0; j < 4; ++j) gr[j] = __builtin_amdgcn_raw_buffer_load_b128(rs, goff + j * 1024, 0, 16);
        __builtin_amdgcn_sched_barrier(0);
        Q_MMA(2) Q_MMA(3)
        f32x4 keep[2];
        {
            f32x4* xp = reinterpret_cast<f32x4*>(smem + L_QX + (w ^ 1) * 2048) + lane;
#pragma unroll
            for (int g = 0; g < 2; ++g) {
                f32x4 lo = {acc[4 * g], acc[4 * g + 1], acc[4 * g + 2], acc[4 * g + 3]};
                f32x4 hi = {acc[8 + 4 * g], acc[9 + 4 * g], acc[10 + 4 * g], acc[11 + 4 * g]};
                xp[g * 64] = kh ? lo : hi;
                keep[g] = kh ? hi : lo;
            }
        }
        const unsigned ka = base + L_K + lane * 16;
        const unsigned va = base + (r < 16 ? L_V + h * 256 + r * 16
                                           : L_CONST + (r == 16 ? 0 : ((r >= 20 && r < 28) ? 64 : 16)));
        const unsigned vstep = r < 16 ? 4096 : 0;
        __builtin_amdgcn_s_setprio(3);
        lds_arrive(cb + C_GATE);
#pragma unroll 1
        for (unsigned spin = 0; spin < SPIN_CAP; ++spin) {
            bool ok = true;
#pragma unroll
            for (int j = 0; j < 4; ++j) ok = ok && gr[j][1] == (unsigned)tag && gr[j][3] == (unsigned)tag;
            if (__all(ok)) break;
            __builtin_amdgcn_s_sleep(2);
#pragma unroll
            for (int j = 0; j < 4; ++j) gr[j] = __builtin_amdgcn_raw_buffer_load_b128(rs, goff + j * 1024, 0, 16);
        }
        if (w == 0) {
#pragma unroll 1
            for (unsigned spin = 0; spin < SPIN_CAP; ++spin) {
                const u32x4 f = __builtin_amdgcn_raw_buffer_load_b128(rs, WS_KVF + (blk & 7) * KVF_REP + lane * 16, 0, 16);
                const unsigned lo = (unsigned)tag, hi = (unsigned)(tag >> 32);
                if (__all(f[0] == lo && f[1] == hi && f[2] == lo && f[3] == hi)) break;
                __builtin_amdgcn_s_sleep(6);
            }
            lds_arrive(cb + C_SEEN);
        }
        lds_wait<1>(cb + C_GATE, 8);
        const f32x4* xq = reinterpret_cast<const f32x4*>(smem + L_QX + w * 2048) + lane;
        const f32x4 qa = (xq[0] + keep[0]) * QSCALE, qb = (xq[64] + keep[1]) * QSCALE;
        const u32x4 q = {pack2(qa[0], qa[1]), pack2(qa[2], qa[3]), pack2(qb[0], qb[1]), pack2(qb[2], qb[3])};
        *reinterpret_cast<u32x4*>(smem + L_K + w * 1024 + lane * 16) = (u32x4){gr[0][0], gr[0][2], gr[1][0], gr[1][2]};
        *reinterpret_cast<u32x4*>(smem + L_V + w * 1024 + lane * 16) = (u32x4){gr[2][0], gr[2][2], gr[3][0], gr[3][2]};
        lds_arrive(cb + C_FIRST);
        lds_wait<0>(cb + C_FIRST, 8);
        float m = 0.f;
        f32x16 o;
        float l;
#pragma unroll 1
        for (int given = 0; given < 2; ++given) {
            o = attn_pass(ka, va, vstep, cb, q, m, given);
            l = __shfl(o[8], r);
            float mag = 0.f;
#pragma unroll
            for (int j = 0; j < 8; ++j) mag += fabsf(o[j]);
            if (!__any(!(l < 1.0e18f) || !(mag < 3.0e38f))) break;
            m = row_max(smem + L_K + lane * 16, q);
        }
        const float inv = 1.0f / l;
        u32x4 of;
#pragma unroll
        for (int j = 0; j < 4; ++j) of[j] = pack2(o[2 * j] * inv, o[2 * j + 1] * inv);
        __builtin_amdgcn_raw_buffer_store_b128(
            of, rs, WS_OF + ((2 * tg + i) * 32 + 4 * hs + hd) * 1024 + lane * 16, 0, 16);
        publish(ws, WS_OFL + ((tg * 8 + hs) * 8 + w) * FL, tag, lane);
        return;
    }

    const int wb4 = w - 8;
    if (kvp) {
        drain();
        lds_arrive(cb + C_BIN);
        if (wb4 < 2) {
            const int tt = 2 * tg + xt, kind = hs >> 2;
            const int c = lane & 15, g = lane >> 4;
            const char* xb = smem + L_X + (32 * xt + 16 * wb4 + c) * 1024;
            const char* wp = smem + L_WKV + (8 * g + tq) * 32 + 8 * tp;
            const int pos = ((g & 1) ? (kind ? 256 : 512) : 0) + (wb4 ? (kind ? 512 : 256) : 0) + c * 16;
            lds_wait<0>(cb + C_BIN, 4);
            const f32x4 a = kind ? kv_proj<false>(xb, wp, g, c) : kv_proj<true>(xb, wp, g, c);
            const unsigned d0 = pack2(a[0], a[1]), d1 = pack2(a[2], a[3]);
            __builtin_amdgcn_raw_buffer_store_b128((u32x4){d0, (unsigned)tag, d1, (unsigned)tag}, rs,
                                                   WS_KVG + tt * 4096 + kind * 2048 + pos + (g >> 1) * 1024, 0, 16);
            __builtin_amdgcn_raw_buffer_store_b64((u32x2){d0, d1}, rs,
                                                  WS_KV + tt * 2048 + kind * 1024 + pos + (g >> 1) * 8, 0, 16);
            lds_arrive(cb + C_KVRD);
            drain();
            if (lds_arrive_count(cb + C_KVST) == 1 && lane < 8)
                __hip_atomic_store((GLBP u64*)(ws + WS_KVF + lane * KVF_REP + (tt * 2 + kind) * 8), tag,
                                   __ATOMIC_RELAXED, __HIP_MEMORY_SCOPE_AGENT);
        }
    }
    lds_wait<1>(cb + C_AIN, UWAIT);
    {
        const __amdgpu_buffer_rsrc_t kr_ = __builtin_amdgcn_make_buffer_rsrc((void*)bk, 0, SEQ * DM * 2, 0x00020000);
        const __amdgpu_buffer_rsrc_t vr_ = __builtin_amdgcn_make_buffer_rsrc((void*)bv, 0, SEQ * DM * 2, 0x00020000);
        const int o0 = (us0 * DM + ud0) * 2;
        int om = o0 - 3072;
        asm volatile("" : "+v"(om));
#pragma unroll
        for (int j = 0; j < 3; ++j) kr[j] = __builtin_amdgcn_raw_buffer_load_b32(kr_, om + 1024 * j, 0, 0);
#pragma unroll
        for (int j = 0; j < 8; ++j) {
            kr[3 + j] = __builtin_amdgcn_raw_buffer_load_b32(kr_, o0 + 1024 * (j & 3), 4096 * (j >> 2), 0);
            vr[j] = __builtin_amdgcn_raw_buffer_load_b32(vr_, o0 + 1024 * (j & 3), 4096 * (j >> 2), 0);
        }
        sk = ld16(shk + ud0 * 4);
    }
    {
        asm volatile("" : "+v"(kr[0]), "+v"(kr[1]), "+v"(kr[2]), "+v"(kr[3]), "+v"(kr[4]), "+v"(kr[5]), "+v"(kr[6]),
                          "+v"(kr[7]), "+v"(kr[8]), "+v"(kr[9]), "+v"(kr[10]), "+v"(sk));
        asm volatile("" : "+v"(vr[0]), "+v"(vr[1]), "+v"(vr[2]), "+v"(vr[3]), "+v"(vr[4]), "+v"(vr[5]), "+v"(vr[6]),
                          "+v"(vr[7]));
        u32x4 ua, ub = {0, 0, 0, 0};
#pragma unroll 1
        for (int e = 0; e < 2; ++e) {
            const unsigned sel = e ? 0x07060c0cu : 0x05040c0cu;
            const unsigned w01 = e ? sk[2] : sk[0], w23 = e ? sk[3] : sk[1];
            const float w0 = bflo(w01), w1 = bfhi(w01), w2 = bflo(w23), w3 = bfhi(w23);
            float kk[11], u[8];
#pragma unroll
            for (int j = 0; j < 11; ++j) kk[j] = __uint_as_float(__builtin_amdgcn_perm(kr[j], 0u, sel));
#pragma unroll
            for (int j = 0; j < 8; j += 2) {
                const unsigned p3 = pack2(kk[j] * w3, kk[j + 1] * w3), p2 = pack2(kk[j + 1] * w2, kk[j + 2] * w2);
                const unsigned p1 = pack2(kk[j + 2] * w1, kk[j + 3] * w1), p0 = pack2(kk[j + 3] * w0, kk[j + 4] * w0);
                unsigned a = pack2(bflo(p3) + bflo(p2), bfhi(p3) + bfhi(p2));
                a = pack2(bflo(a) + bflo(p1), bfhi(a) + bfhi(p1));
                a = pack2(bflo(a) + bflo(p0), bfhi(a) + bfhi(p0));
                u[j] = bflo(a) * __uint_as_float(__builtin_amdgcn_perm(vr[j], 0u, sel));
                u[j + 1] = bfhi(a) * __uint_as_float(__builtin_amdgcn_perm(vr[j + 1], 0u, sel));
            }
            ua = ub;
            ub = (u32x4){pack2(u[7], u[6]), pack2(u[5], u[4]), pack2(u[3], u[2]), pack2(u[1], u[0])};
        }
        lds_wait<1>(cb + C_SEEN, 1);
        lds_wait<1>(cb + C_GATE, 8);
        const int s0 = wb4 < 2 ? 8 : 40, ns = wb4 < 2 ? 32 : 24;
        const char* src = ws + WS_KV + (wb4 & 1) * 1024;
        const unsigned dst = base + ((wb4 & 1) ? L_V : L_K);
#pragma unroll 1
        for (int t = s0; t < s0 + ns; ++t) dma16_dev(src + ((t + rot) & 63) * 2048, lane * 16, dst + t * 1024);
        if (wb4 < 2) {
            asm volatile("s_waitcnt vmcnt(24)" ::: "memory");
            lds_arrive(cb + C_NEXT);
        }
        __builtin_amdgcn_raw_buffer_store_b128(ua, rs, WS_UR + (ud0 * SEQ + (SEQ - 8) - us0) * 2, 0, 16);
        __builtin_amdgcn_raw_buffer_store_b128(ub, rs, WS_UR + (ud0 * SEQ + (SEQ - 8) - us0) * 2, SEQ * 2, 16);
        drain();
        lds_arrive(cb + (wb4 < 2 ? C_MID : C_LAST));
        if (lds_arrive_count(cb + C_UR) == 3) {
            lds_wait<1>(cb + C_TAG, 8);
            if (lane == 0)
                __hip_atomic_store((GLBP u64*)(ws + WS_URF + (hs * 32 + tg) * 8), tag, __ATOMIC_RELAXED, __HIP_MEMORY_SCOPE_AGENT);
        }
        lds_wait<1>(cb + C_FIRST, 8);
    }

    {
        const int ch = wb4 >> 1, sh = wb4 & 1;
        const int d = 2 * blk + ch;
        const float dd = bf2f(ssmd[d]);
        if (wb4 == 0) {
            __builtin_amdgcn_s_sleep(USLEEP);
            wait_flags<8>(ws, WS_URF + ((d >> 6) * 32 + lane) * 8, lane < 32, tag);
            lds_arrive(cb + C_URS);
        }
        lds_wait<4>(cb + C_URS, 1);
        char* ul = smem + L_U + ch * 2 * U_RS;
        u32x4 uv[4];
#pragma unroll
        for (int j = 0; j < 4; ++j)
            uv[j] = __builtin_amdgcn_raw_buffer_load_b128(rs, WS_UR + d * 4096 + j * 1024 + lane * 16 + 2 * sh, 0, 16);
        if (sh && lane == 63) uv[3][3] &= 0xffffu;
#pragma unroll
        for (int j = 0; j < 4; ++j)
            *reinterpret_cast<u32x4*>(ul + sh * U_RS + j * 1024 + lane * 16) = uv[j];
        if (lane < 2) *reinterpret_cast<u32x4*>(ul + sh * U_RS + 4096 + lane * 16) = (u32x4){0, 0, 0, 0};
        lds_arrive(cb + C_U + 4 * ch);
        lds_wait<1>(cb + C_U + 4 * ch, 2);
        const __amdgpu_buffer_rsrc_t hk = __builtin_amdgcn_make_buffer_rsrc((void*)(ssmk + (size_t)d * SEQ), 0, SEQ * 2, 0x00020000);
        if (sh) conv_wave<1, 2, 4, 7>(hk, ul, rs, WS_ZT + d * 4096, dd, lane);
        else conv_wave<0, 3, 5, 6>(hk, ul, rs, WS_ZT + d * 4096, dd, lane);
        publish(ws, WS_ZF + (((d >> 6) * 2 + sh) * 64 + (d & 63)) * 8, tag, lane);
    }

    if (blk == 0 && wb4 == 2) {
#pragma unroll 1
        for (int j = 0; j < 4; ++j) wait_flags<24>(ws, WS_URF + (j * 64 + lane) * 8, true, tag);
        if (lane == 0) __hip_atomic_store((GLBP u64*)(ws + WS_EPOCH), tag, __ATOMIC_RELAXED, __HIP_MEMORY_SCOPE_AGENT);
    }

    u32x4 fe[8], fo[8];
    {
        const __amdgpu_buffer_rsrc_t wr = __builtin_amdgcn_make_buffer_rsrc((void*)wo, 0, DM * DM * 2, 0x00020000);
        const int vo = (4 * h * DM + 2 * (r ^ (8 * wb4))) * 2;
        const int so = (128 * wb4 * DM + 64 * hs) * 2;
        unsigned wd[8][8];
#pragma unroll
        for (int s = 0; s < 8; ++s)
#pragma unroll
            for (int j = 0; j < 8; ++j)
                wd[s][j] = __builtin_amdgcn_raw_buffer_load_b32(wr, vo + (j & 3) * 1024, so + (16 * s + 8 * (j >> 2)) * 1024, 0);
        __builtin_amdgcn_sched_barrier(0);
#pragma unroll
        for (int s = 0; s < 8; ++s) {
#pragma unroll
            for (int j = 0; j < 4; ++j) {
                fe[s][j] = __builtin_amdgcn_perm(wd[s][2 * j + 1], wd[s][2 * j], 0x05040100u);
                fo[s][j] = __builtin_amdgcn_perm(wd[s][2 * j + 1], wd[s][2 * j], 0x07060302u);
            }
            asm volatile("" : "+v"(fe[s]), "+v"(fo[s]));
        }
    }
    {
        const int s0 = 64 * tg + 16 * wb4 + 2 * (lane >> 3), d0 = 64 * hs + 8 * (lane & 7);
        const u32x4 q0 = ld16(bq + (size_t)s0 * DM + d0), q1 = ld16(bq + (size_t)(s0 + 1) * DM + d0);
        if (wb4 == 0) {
            wait_flags<16>(ws, WS_ZF + ((hs * 2 + ((0x96 >> (tg >> 2)) & 1)) * 64 + lane) * 8, true, tag);
            lds_arrive(cb + C_Z);
        }
        lds_wait<4>(cb + C_Z, 1);
        unsigned z[8];
#pragma unroll
        for (int e = 0; e < 8; ++e)
            z[e] = __builtin_amdgcn_raw_buffer_load_b32(rs, (d0 * SEQ + s0) * 2, WS_ZT + e * SEQ * 2, 16);
        __builtin_amdgcn_sched_barrier(0);
        u32x4 o0, o1;
#pragma unroll
        for (int j = 0; j < 4; ++j) {
            o0[j] = pack2(bflo(q0[j]) * bflo(z[2 * j]), bfhi(q0[j]) * bflo(z[2 * j + 1]));
            o1[j] = pack2(bflo(q1[j]) * bfhi(z[2 * j]), bfhi(q1[j]) * bfhi(z[2 * j + 1]));
        }
        __builtin_amdgcn_raw_buffer_store_b128(o0, ro, (s0 * 1024 + 512 + d0) * 2, 0, OUT_AUX);
        __builtin_amdgcn_raw_buffer_store_b128(o1, ro, (s0 * 1024 + 512 + d0) * 2 + 2048, 0, OUT_AUX);
    }
    lds_arrive(cb + C_TAIL);
    lds_wait<8>(cb + C_NEAR, 4);
#pragma unroll 1
    for (int i = 0; i < 2; ++i) {
        wait_flags<1>(ws, WS_OFL + ((tg * 8 + 2 * wb4 + (lane >> 2)) * 8 + 4 * i + (lane & 3)) * FL, lane < 8, tag);
        u32x4 of[8];
#pragma unroll
        for (int s = 0; s < 8; ++s)
            of[s] = __builtin_amdgcn_raw_buffer_load_b128(
                rs, lane * 16 + (s & 3) * 1024, WS_OF + ((2 * tg + i) * 32 + 8 * wb4 + (s & 4)) * 1024, 16);
        __builtin_amdgcn_sched_barrier(0);
        f32x16 ae = {0}, ao = {0};
#pragma unroll
        for (int s = 0; s < 8; ++s) {
            ae = mfma32(fe[s], of[s], ae);
            ao = mfma32(fo[s], of[s], ao);
        }
        lds_wait<0>(cb + C_TAIL, 4 + 8 * i);
#pragma unroll
        for (int g = 1; g < 4; ++g) {
            f32x4* p = reinterpret_cast<f32x4*>(smem + L_RED + ((g ^ wb4) * 3 + g - 1) * 2048) + lane;
            p[0] = (f32x4){ae[4 * g], ae[4 * g + 1], ae[4 * g + 2], ae[4 * g + 3]};
            p[64] = (f32x4){ao[4 * g], ao[4 * g + 1], ao[4 * g + 2], ao[4 * g + 3]};
        }
        lds_arrive(cb + C_TAIL);
        lds_wait<0>(cb + C_TAIL, 8 + 8 * i);
        f32x4 e = {ae[0], ae[1], ae[2], ae[3]}, o = {ao[0], ao[1], ao[2], ao[3]};
        const f32x4* p = reinterpret_cast<const f32x4*>(smem + L_RED + wb4 * 3 * 2048) + lane;
#pragma unroll
        for (int g = 0; g < 3; ++g) {
            e += p[g * 128];
            o += p[g * 128 + 64];
        }
        const u32x4 res = {pack2(e[0], o[0]), pack2(e[1], o[1]), pack2(e[2], o[2]), pack2(e[3], o[3])};
        __builtin_amdgcn_raw_buffer_store_b128(res, ro, ((64 * tg + 32 * i + r) * 1024 + 64 * hs + 16 * wb4 + 8 * h) * 2, 0, OUT_AUX);
        lds_arrive(cb + C_TAIL);
    }
}

extern "C" void kernel_launch(void* const* d_in, const int* in_sizes, int n_in, void* d_out,
                              int out_size, void* d_ws, size_t ws_size, hipStream_t stream) {
    (void)hipFuncSetAttribute((const void*)fused_kernel, hipFuncAttributeMaxDynamicSharedMemorySize, LDS_BYTES);
    fused_kernel<<<256, 768, LDS_BYTES, stream>>>(
        (const u16*)d_in[0], (const u16*)d_in[1], (const u16*)d_in[2], (const u16*)d_in[3],
        (const u16*)d_in[4], (const u16*)d_in[5], (const u16*)d_in[6], (const u16*)d_in[7],
        (const u16*)d_in[8], (const u16*)d_in[9], (const u16*)d_in[10], (u16*)d_out, (char*)d_ws);
}
```
